```python
import torch

def _softmax(x, axis=-1):
    return torch.softmax(x, dim=axis)
def _gelu(x):
    return torch.nn.functional.gelu(x, approximate="tanh")
def _silu(x):
    return torch.nn.functional.silu(x)
def _sigmoid(x):
    return torch.sigmoid(x)
def _layernorm(x, axis=-1, eps=1e-5):
    m = x.mean(dim=axis, keepdim=True)
    v = x.var(dim=axis, keepdim=True, unbiased=False)
    return (x - m) / torch.sqrt(v + eps)
def _scatter_add(tgt, idx, src, axis=0):
    return tgt.clone().index_add_(axis, idx, src)
def _segment_sum(src, seg, n):
    out = torch.zeros((n,) + src.shape[1:], dtype=src.dtype)
    return out.index_add_(0, seg, src)
def _pad(x, pw):
    flat = tuple(v for pair in reversed(pw) for v in pair)
    return torch.nn.functional.pad(x, flat)

kernel_name = 'parallel_sum__gqa_block_cached__geglu_ffn__decode'

def setup_inputs():

    _w = (lambda a: (lambda t: t.to(torch.bfloat16) if t.is_floating_point() else t)(torch.as_tensor(a)))
    def _A_setup():
        B, Sq, D = 2, 1, 448
        Hq, Hkv, Sk, Dh = 16, 8, 32768, 256
        assert Hq % Hkv == 0, (Hq, Hkv)
        return {
            "x": _w((lambda *s: torch.randn(*s, dtype=torch.bfloat16) * 0.05)(B, Sq, D)),
            "k_cache": _w((lambda *s: torch.randn(*s, dtype=torch.bfloat16) * 3.2)(B, Hkv, Sk, Dh)),
            "v_cache": _w((lambda *s: torch.randn(*s, dtype=torch.bfloat16) * 1.6)(B, Hkv, Sk, Dh)),
            "wq": _w((lambda *s: torch.randn(*s, dtype=torch.bfloat16) * 3.2)(D, Hq * Dh) * (D ** -0.5)),
            "wo": _w((lambda *s: torch.randn(*s, dtype=torch.bfloat16) * 1.6)(Hq * Dh, D) * ((Hq * Dh) ** -0.5)),
        }
    _A_in = _A_setup()

    def _B_setup():
        B, S, D, F = 2, 1, 448, 1792
        return {
            "x": _w((lambda *s: torch.randn(*s, dtype=torch.bfloat16) * 0.05)(B, S, D)),
            "w_gate": _w((lambda *s: torch.randn(*s, dtype=torch.bfloat16) * 0.05)(D, F)),
            "w_up": _w((lambda *s: torch.randn(*s, dtype=torch.bfloat16) * 0.05)(D, F)),
            "w_down": _w((lambda *s: torch.randn(*s, dtype=torch.bfloat16) * 0.05)(F, D)),
        }
    _B_in = _B_setup()

    return {
        "A_x": _A_in["x"],
        "A_k_cache": _A_in["k_cache"],
        "A_v_cache": _A_in["v_cache"],
        "A_wq": _A_in["wq"],
        "A_wo": _A_in["wo"],
        "B_x": _B_in["x"],
        "B_w_gate": _B_in["w_gate"],
        "B_w_up": _B_in["w_up"],
        "B_w_down": _B_in["w_down"],
    }

def reference(A_x, A_k_cache, A_v_cache, A_wq, A_wo, B_x, B_w_gate, B_w_up, B_w_down):
    def _A_ref(x, k_cache, v_cache, wq, wo):
        B, Sq, D = x.shape
        Hq, Dh = 16, 256
        Hkv = k_cache.shape[1]
        rep = Hq // Hkv
        q = (lambda x, *d: x.permute(*d))((x @ wq).reshape(B, Sq, Hq, Dh), 0, 2, 1, 3)
        k = (lambda x, n, axis: torch.repeat_interleave(x, n, dim=axis))(k_cache, rep, axis=1)
        v = (lambda x, n, axis: torch.repeat_interleave(x, n, dim=axis))(v_cache, rep, axis=1)
        scores = torch.einsum("bhqd,bhkd->bhqk", q, k) * (Dh ** -0.5)
        p = _softmax(scores, axis=-1)
        o = torch.einsum("bhqk,bhkd->bhqd", p, v)
        return (lambda x, *d: x.permute(*d))(o, 0, 2, 1, 3).reshape(B, Sq, Hq * Dh) @ wo

    def _B_ref(x, w_gate, w_up, w_down):
        return (_gelu(x @ w_gate) * (x @ w_up)) @ w_down

    x = A_x
    xn = _layernorm(x, axis=-1)
    return x + _A_ref(xn, A_k_cache, A_v_cache, A_wq, A_wo) + _B_ref(xn, B_w_gate, B_w_up, B_w_down)
```

```cpp
#include <hip/hip_runtime.h>
#include <hip/hip_bf16.h>
#include <stdint.h>

typedef unsigned int u32;
typedef unsigned short u16;
using u32x4 = __attribute__((ext_vector_type(4))) unsigned;
using f32x4 = __attribute__((ext_vector_type(4))) float;
using bf16x2 = __attribute__((ext_vector_type(2))) __bf16;

constexpr int NB = 2, HQ = 16, HKV = 8, SK = 32768, DH = 256, DM = 448, FF = 1792;
constexpr int NQ = HQ * DH;
constexpr int NSPLIT = 16;
constexpr int KEYS_PER_SPLIT = SK / NSPLIT;
constexpr int AW = 8;
constexpr int STAGE = 16;
constexpr int NSTAGE = KEYS_PER_SPLIT / (AW * STAGE);
constexpr float LOG2E = 1.4426950408889634f;

constexpr size_t WS_CNT = 0;
constexpr size_t WS_Q = 4096;
constexpr size_t WS_H = WS_Q + (size_t)NB * NQ * 2;
constexpr size_t WS_PML = 32768;
constexpr size_t WS_PO = 65536;
constexpr size_t WS_SLAB = WS_PO + (size_t)NB * HQ * NSPLIT * DH * 4;

__device__ __forceinline__ float bfr(float x) { return (float)(__bf16)x; }
__device__ __forceinline__ float bf_lo(u32 v) { return __uint_as_float(v << 16); }
__device__ __forceinline__ float bf_hi(u32 v) { return __uint_as_float(v & 0xffff0000u); }
__device__ __forceinline__ float bf2f(u16 v) { return __uint_as_float((u32)v << 16); }
__device__ __forceinline__ u16 f2bf(float x) { __bf16 b = (__bf16)x; return __builtin_bit_cast(u16, b); }
__device__ __forceinline__ float dot2(u32 k, u32 q, float acc) {
  return __builtin_amdgcn_fdot2_f32_bf16(__builtin_bit_cast(bf16x2, k), __builtin_bit_cast(bf16x2, q), acc, false);
}
template <int CTRL> __device__ __forceinline__ float dpp(float x) {
  return __builtin_bit_cast(float, __builtin_amdgcn_mov_dpp(__builtin_bit_cast(int, x), CTRL, 0xf, 0xf, true));
}
constexpr int XOR1 = 0xB1, XOR2 = 0x4E, XOR7 = 0x141, XOR8 = 0x128, ROR4 = 0x124;

__device__ __forceinline__ void layernorm_to_lds(const u16* __restrict__ x, float* xn  , int tid) {
  const int wave = tid >> 6, lane = tid & 63;
  if (wave < NB) {
    float v[7];
    float s = 0.f;
#pragma unroll
    for (int i = 0; i < 7; ++i) { v[i] = bf2f(x[wave * DM + lane + 64 * i]); s += v[i]; }
#pragma unroll
    for (int d = 32; d > 0; d >>= 1) s += __shfl_xor(s, d, 64);
    const float mean = s * (1.0f / DM);
    float q = 0.f;
#pragma unroll
    for (int i = 0; i < 7; ++i) { const float t = v[i] - mean; q += t * t; }
#pragma unroll
    for (int d = 32; d > 0; d >>= 1) q += __shfl_xor(q, d, 64);
    const float m_bf = bfr(mean);
    const float var_bf = bfr(q * (1.0f / DM));
    const float den = bfr(sqrtf(bfr(var_bf + 1e-5f)));
#pragma unroll
    for (int i = 0; i < 7; ++i) xn[wave * DM + lane + 64 * i] = bfr(bfr(v[i] - m_bf) / den);
  }
}

constexpr int K1_QWG = NQ / 32, K1_FWG = FF / 16;
__global__ void __launch_bounds__(256) k1_proj(const u16* __restrict__ x, const u16* __restrict__ wq,
                                               const u16* __restrict__ wg, const u16* __restrict__ wu,
                                               unsigned char* __restrict__ ws) {
  __shared__ float xn[NB * DM];
  __shared__ float red[4][4][16];
  const int tid = threadIdx.x, lane = tid & 63, wave = tid >> 6;
  const int wgid = blockIdx.x;
  const int cq = tid & 3, rg = tid >> 2;
  const bool isq = wgid < K1_QWG;
  const u16* wbase; int ld;
  if (isq) { wbase = wq + wgid * 32 + cq * 8; ld = NQ; }
  else { const int f = wgid - K1_QWG; wbase = ((cq & 2) ? wu : wg) + f * 16 + (cq & 1) * 8; ld = FF; }
  u32x4 w[7];
#pragma unroll
  for (int i = 0; i < 7; ++i) w[i] = *reinterpret_cast<const u32x4*>(wbase + (size_t)(rg + 64 * i) * ld);
  if (wgid == 0 && tid < 8) reinterpret_cast<u32*>(ws + WS_CNT)[tid * 16] = 0u;
  layernorm_to_lds(x, xn, tid);
  __syncthreads();
  float acc[2][8];
#pragma unroll
  for (int b = 0; b < 2; ++b)
#pragma unroll
    for (int j = 0; j < 8; ++j) acc[b][j] = 0.f;
#pragma unroll
  for (int i = 0; i < 7; ++i) {
    const float x0 = xn[rg + 64 * i], x1 = xn[DM + rg + 64 * i];
#pragma unroll
    for (int j = 0; j < 4; ++j) {
      const float lo = bf_lo(w[i][j]), hi = bf_hi(w[i][j]);
      acc[0][2 * j] = fmaf(x0, lo, acc[0][2 * j]); acc[0][2 * j + 1] = fmaf(x0, hi, acc[0][2 * j + 1]);
      acc[1][2 * j] = fmaf(x1, lo, acc[1][2 * j]); acc[1][2 * j + 1] = fmaf(x1, hi, acc[1][2 * j + 1]);
    }
  }
#pragma unroll
  for (int b = 0; b < 2; ++b)
#pragma unroll
    for (int j = 0; j < 8; ++j) {
      float t = acc[b][j];
      t += __shfl_xor(t, 4, 64); t += __shfl_xor(t, 8, 64); t += __shfl_xor(t, 16, 64); t += __shfl_xor(t, 32, 64);
      acc[b][j] = t;
    }
  if (lane < 4) {
#pragma unroll
    for (int b = 0; b < 2; ++b)
#pragma unroll
      for (int j = 0; j < 8; ++j) red[wave][lane][b * 8 + j] = acc[b][j];
  }
  __syncthreads();
  if (tid < 64) {
    const int c4 = tid >> 4, idx = tid & 15;
    const float t = (red[0][c4][idx] + red[1][c4][idx]) + (red[2][c4][idx] + red[3][c4][idx]);
    if (isq) {
      const int b = idx >> 3, j = idx & 7;
      reinterpret_cast<u16*>(ws + WS_Q)[b * NQ + wgid * 32 + c4 * 8 + j] = f2bf(t);
    } else {
      const float up = __shfl_down(t, 32, 64);
      if (tid < 32) {
        const int b = idx >> 3, j = idx & 7;
        const float g = bfr(t), u = bfr(up);
        const float inner = 0.7978845608028654f * (g + 0.044715f * g * g * g);
        const float ge = bfr(0.5f * g * (1.0f + tanhf(inner)));
        reinterpret_cast<u16*>(ws + WS_H)[b * FF + (wgid - K1_QWG) * 16 + c4 * 8 + j] = f2bf(ge * u);
      }
    }
  }
}

struct Stg { u32x4 k[8]; u32x4 v[8]; };

__device__ __forceinline__ void stg_issue(Stg& s, const u32x4* __restrict__ kp, const u32x4* __restrict__ vp) {
#pragma unroll
  for (int i = 0; i < 8; ++i) s.k[i] = __builtin_nontemporal_load(kp + i * 64);
#pragma unroll
  for (int i = 0; i < 8; ++i) s.v[i] = __builtin_nontemporal_load(vp + i * 64);
}

__device__ __forceinline__ void stg_compute(const Stg& s, const u32x4 (&q)[2], float (&acc)[2][8], float& m, float& l,
                                            float* prow  , int lane) {
  const int r = lane >> 5, c = lane & 31;
  float x[16];
#pragma unroll
  for (int i = 0; i < 8; ++i)
#pragma unroll
    for (int g = 0; g < 2; ++g) {
      float d = 0.f;
#pragma unroll
      for (int k = 0; k < 4; ++k) d = dot2(s.k[i][k], q[g][k], d);
      x[i * 2 + g] = d;
    }
#pragma unroll
  for (int j = 0; j < 8; ++j) {
    auto t = __builtin_amdgcn_permlane16_swap(__float_as_uint(x[j]), __float_as_uint(x[j + 8]), false, false);
    x[j] = __uint_as_float(t[0]) + __uint_as_float(t[1]);
  }
#define TR_STEP(HALF, CTRL, BIT) _Pragma("unroll") for (int h = 0; h < (HALF); ++h) { \
    const float lo_ = x[h], hi_ = x[h + (HALF)]; const float t1_ = lo_ + dpp<CTRL>(lo_), t2_ = hi_ + dpp<CTRL>(hi_); \
    x[h] = (c & (BIT)) ? t2_ : t1_; }
  TR_STEP(4, XOR8, 8) TR_STEP(2, XOR7, 4) TR_STEP(1, XOR2, 2)
#undef TR_STEP
  float sc = x[0] + dpp<XOR1>(x[0]);
  sc = bfr(sc) * (0.0625f * LOG2E);
  float pm = sc;
  pm = fmaxf(pm, dpp<ROR4>(pm)); pm = fmaxf(pm, dpp<XOR8>(pm));
  {
    auto t = __builtin_amdgcn_permlane16_swap(__float_as_uint(pm), __float_as_uint(pm), false, false);
    pm = fmaxf(__uint_as_float(t[0]), __uint_as_float(t[1]));
    auto u = __builtin_amdgcn_permlane32_swap(__float_as_uint(pm), __float_as_uint(pm), false, false);
    pm = fmaxf(__uint_as_float(u[0]), __uint_as_float(u[1]));
  }
  const float mn = fmaxf(m, pm);
  const float alpha = __builtin_amdgcn_exp2f(m - mn);
  const float p = __builtin_amdgcn_exp2f(sc - mn);
  l = fmaf(l, alpha, p);
  m = mn;
  const float a0 = dpp<0x00>(alpha), a1 = dpp<0xAA>(alpha);
#pragma unroll
  for (int k = 0; k < 8; ++k) { acc[0][k] *= a0; acc[1][k] *= a1; }
  prow[r * 16 + (c >> 1)] = p;
  f32x4 pr[4];
#pragma unroll
  for (int u = 0; u < 4; ++u) pr[u] = *reinterpret_cast<const f32x4*>(prow + r * 16 + 4 * u);
#pragma unroll
  for (int i = 0; i < 8; ++i) {
    const float p0 = pr[i >> 1][(i & 1) * 2], p1 = pr[i >> 1][(i & 1) * 2 + 1];
#pragma unroll
    for (int k = 0; k < 4; ++k) {
      const float lo = bf_lo(s.v[i][k]), hi = bf_hi(s.v[i][k]);
      acc[0][2 * k] = fmaf(p0, lo, acc[0][2 * k]); acc[0][2 * k + 1] = fmaf(p0, hi, acc[0][2 * k + 1]);
      acc[1][2 * k] = fmaf(p1, lo, acc[1][2 * k]); acc[1][2 * k + 1] = fmaf(p1, hi, acc[1][2 * k + 1]);
    }
  }
}

__global__ void __launch_bounds__(AW * 64, 2) k2_attn(const u16* __restrict__ kc, const u16* __restrict__ vc,
                                                      unsigned char* __restrict__ ws) {
  __shared__ __attribute__((aligned(16))) float lds_p[AW][32];
  __shared__ __attribute__((aligned(16))) float lds_acc[AW][2][DH];
  __shared__ float lds_ml[AW][2][2];
  const int tid = threadIdx.x, lane = tid & 63, wave = __builtin_amdgcn_readfirstlane(tid >> 6);
  const int split = blockIdx.x % NSPLIT, bh = blockIdx.x / NSPLIT;
  const int b = bh / HKV, kvh = bh % HKV;
  const int c = lane & 31;
  const size_t base = ((size_t)bh * SK + (size_t)split * KEYS_PER_SPLIT + (size_t)wave * STAGE) * DH;
  const u32x4* kp = reinterpret_cast<const u32x4*>(kc + base) + lane;
  const u32x4* vp = reinterpret_cast<const u32x4*>(vc + base) + lane;
  constexpr int SSTRIDE = AW * STAGE * DH / 8;

  Stg A, Bf;
  stg_issue(A, kp, vp);
  u32x4 q[2];
#pragma unroll
  for (int g = 0; g < 2; ++g)
    q[g] = *reinterpret_cast<const u32x4*>(reinterpret_cast<const u16*>(ws + WS_Q) + b * NQ + (kvh * 2 + g) * DH + 8 * c);
  float acc[2][8];
#pragma unroll
  for (int g = 0; g < 2; ++g)
#pragma unroll
    for (int k = 0; k < 8; ++k) acc[g][k] = 0.f;
  float m = -INFINITY, l = 0.f;
  float* prow = &lds_p[wave][0];
#pragma unroll 1
  for (int j = 0; j < NSTAGE; j += 2) {
    stg_issue(Bf, kp + (size_t)(j + 1) * SSTRIDE, vp + (size_t)(j + 1) * SSTRIDE);
    stg_compute(A, q, acc, m, l, prow, lane);
    if (j + 2 < NSTAGE) stg_issue(A, kp + (size_t)(j + 2) * SSTRIDE, vp + (size_t)(j + 2) * SSTRIDE);
    stg_compute(Bf, q, acc, m, l, prow, lane);
  }
#pragma unroll
  for (int g = 0; g < 2; ++g)
#pragma unroll
    for (int k = 0; k < 8; ++k) {
      auto u = __builtin_amdgcn_permlane32_swap(__float_as_uint(acc[g][k]), __float_as_uint(acc[g][k]), false, false);
      acc[g][k] = __uint_as_float(u[0]) + __uint_as_float(u[1]);
    }
  {
    float t = l;
    t += dpp<ROR4>(t); t += dpp<XOR8>(t);
    auto s1 = __builtin_amdgcn_permlane16_swap(__float_as_uint(t), __float_as_uint(t), false, false);
    t = __uint_as_float(s1[0]) + __uint_as_float(s1[1]);
    auto s2 = __builtin_amdgcn_permlane32_swap(__float_as_uint(t), __float_as_uint(t), false, false);
    l = __uint_as_float(s2[0]) + __uint_as_float(s2[1]);
  }
  if (lane < 32) {
#pragma unroll
    for (int g = 0; g < 2; ++g) {
      *reinterpret_cast<f32x4*>(&lds_acc[wave][g][8 * c]) = f32x4{acc[g][0], acc[g][1], acc[g][2], acc[g][3]};
      *reinterpret_cast<f32x4*>(&lds_acc[wave][g][8 * c + 4]) = f32x4{acc[g][4], acc[g][5], acc[g][6], acc[g][7]};
    }
    if (c == 0 || c == 2) { lds_ml[wave][c >> 1][0] = m; lds_ml[wave][c >> 1][1] = l; }
  }
  __syncthreads();
  {
    const int g = tid >> 8, d = tid & 255;
    float M = -INFINITY;
#pragma unroll
    for (int w = 0; w < AW; ++w) M = fmaxf(M, lds_ml[w][g][0]);
    float L = 0.f, o = 0.f;
#pragma unroll
    for (int w = 0; w < AW; ++w) {
      const float wt = __builtin_amdgcn_exp2f(lds_ml[w][g][0] - M);
      L = fmaf(wt, lds_ml[w][g][1], L);
      o = fmaf(wt, lds_acc[w][g][d], o);
    }
    const size_t row = ((size_t)b * HQ + kvh * 2 + g) * NSPLIT + split;
    reinterpret_cast<float*>(ws + WS_PO)[row * DH + d] = o;
    if (d == 0) { reinterpret_cast<float*>(ws + WS_PML)[row * 2] = M; reinterpret_cast<float*>(ws + WS_PML)[row * 2 + 1] = L; }
  }
}

constexpr int K3_CG = 7, K3_KC = 23;
__global__ void __launch_bounds__(256) k3_out(const u16* __restrict__ x, const u16* __restrict__ wo,
                                              const u16* __restrict__ wd, unsigned char* __restrict__ ws,
                                              u16* __restrict__ out) {
  __shared__ float act[2][DH];
  __shared__ float red[4][8][16];
  __shared__ u32 s_last;
  const int tid = threadIdx.x, lane = tid & 63, wave = tid >> 6;
  const int cg = blockIdx.x % K3_CG, kcx = blockIdx.x / K3_CG;
  const int cq = tid & 7, rg = tid >> 3;
  const u16* wbase = (kcx < HQ ? wo + (size_t)kcx * DH * DM : wd + (size_t)(kcx - HQ) * DH * DM) + cg * 64 + cq * 8;
  u32x4 w[8];
#pragma unroll
  for (int i = 0; i < 8; ++i) w[i] = *reinterpret_cast<const u32x4*>(wbase + (size_t)(rg + 32 * i) * DM);
  if (kcx < HQ) {
    const float* pml = reinterpret_cast<const float*>(ws + WS_PML);
    const float* po = reinterpret_cast<const float*>(ws + WS_PO);
#pragma unroll
    for (int b = 0; b < 2; ++b) {
      const size_t row0 = ((size_t)b * HQ + kcx) * NSPLIT;
      float a[NSPLIT];
#pragma unroll
      for (int s = 0; s < NSPLIT; ++s) a[s] = po[(row0 + s) * DH + tid];
      float M = -INFINITY;
#pragma unroll
      for (int s = 0; s < NSPLIT; ++s) M = fmaxf(M, pml[(row0 + s) * 2]);
      float L = 0.f, o = 0.f;
#pragma unroll
      for (int s = 0; s < NSPLIT; ++s) {
        const float wt = __builtin_amdgcn_exp2f(pml[(row0 + s) * 2] - M);
        L = fmaf(wt, pml[(row0 + s) * 2 + 1], L);
        o = fmaf(wt, a[s], o);
      }
      act[b][tid] = bfr(o / L);
    }
  } else {
    const u16* h = reinterpret_cast<const u16*>(ws + WS_H);
#pragma unroll
    for (int b = 0; b < 2; ++b) act[b][tid] = bf2f(h[b * FF + (kcx - HQ) * DH + tid]);
  }
  __syncthreads();
  float acc[2][8];
#pragma unroll
  for (int b = 0; b < 2; ++b)
#pragma unroll
    for (int j = 0; j < 8; ++j) acc[b][j] = 0.f;
#pragma unroll
  for (int i = 0; i < 8; ++i) {
    const float x0 = act[0][rg + 32 * i], x1 = act[1][rg + 32 * i];
#pragma unroll
    for (int j = 0; j < 4; ++j) {
      const float lo = bf_lo(w[i][j]), hi = bf_hi(w[i][j]);
      acc[0][2 * j] = fmaf(x0, lo, acc[0][2 * j]); acc[0][2 * j + 1] = fmaf(x0, hi, acc[0][2 * j + 1]);
      acc[1][2 * j] = fmaf(x1, lo, acc[1][2 * j]); acc[1][2 * j + 1] = fmaf(x1, hi, acc[1][2 * j + 1]);
    }
  }
#pragma unroll
  for (int b = 0; b < 2; ++b)
#pragma unroll
    for (int j = 0; j < 8; ++j) {
      float t = acc[b][j];
      t += __shfl_xor(t, 8, 64); t += __shfl_xor(t, 16, 64); t += __shfl_xor(t, 32, 64);
      acc[b][j] = t;
    }
  if (lane < 8) {
#pragma unroll
    for (int b = 0; b < 2; ++b)
#pragma unroll
      for (int j = 0; j < 8; ++j) red[wave][lane][b * 8 + j] = acc[b][j];
  }
  __syncthreads();
  float* slab = reinterpret_cast<float*>(ws + WS_SLAB) + (size_t)cg * K3_KC * 128;
  const int ob = tid >> 6, oc = tid & 63;
  if (tid < 128) {
    const int c8 = oc >> 3, j = oc & 7;
    const float t = (red[0][c8][ob * 8 + j] + red[1][c8][ob * 8 + j]) + (red[2][c8][ob * 8 + j] + red[3][c8][ob * 8 + j]);
    __hip_atomic_store(slab + kcx * 128 + tid, t, __ATOMIC_RELAXED, __HIP_MEMORY_SCOPE_AGENT);
  }
  asm volatile("s_waitcnt vmcnt(0)" ::: "memory");
  __syncthreads();
  if (tid == 0) {
    u32* cnt = reinterpret_cast<u32*>(ws + WS_CNT) + cg * 16;
    const u32 old = __hip_atomic_fetch_add(cnt, 1u, __ATOMIC_RELAXED, __HIP_MEMORY_SCOPE_AGENT);
    s_last = (old == (u32)(K3_KC - 1)) ? 1u : 0u;
  }
  __syncthreads();
  if (s_last == 0u) return;
  if (tid < 128) {
    float v[K3_KC];
#pragma unroll
    for (int k = 0; k < K3_KC; ++k) v[k] = __hip_atomic_load(slab + k * 128 + tid, __ATOMIC_RELAXED, __HIP_MEMORY_SCOPE_AGENT);
    float sa = 0.f, sf = 0.f;
#pragma unroll
    for (int k = 0; k < HQ; ++k) sa += v[k];
#pragma unroll
    for (int k = HQ; k < K3_KC; ++k) sf += v[k];
    const int n = cg * 64 + oc;
    const float xv = bf2f(x[ob * DM + n]);
    out[ob * DM + n] = f2bf(bfr(xv + bfr(sa)) + bfr(sf));
  }
}

extern "C" void kernel_launch(void* const* d_in, const int* in_sizes, int n_in,
                              void* d_out, int out_size, void* d_ws, size_t ws_size,
                              hipStream_t stream) {
  const u16* x = (const u16*)d_in[0];
  const u16* kc = (const u16*)d_in[1];
  const u16* vc = (const u16*)d_in[2];
  const u16* wq = (const u16*)d_in[3];
  const u16* wo = (const u16*)d_in[4];
  const u16* wg = (const u16*)d_in[6];
  const u16* wu = (const u16*)d_in[7];
  const u16* wd = (const u16*)d_in[8];
  unsigned char* ws = (unsigned char*)d_ws;
  k1_proj<<<K1_QWG + K1_FWG, 256, 0, stream>>>(x, wq, wg, wu, ws);
  k2_attn<<<NB * HKV * NSPLIT, AW * 64, 0, stream>>>(kc, vc, ws);
  k3_out<<<K3_CG * K3_KC, 256, 0, stream>>>(x, wo, wd, ws, (u16*)d_out);
}
```

```cpp
#include <hip/hip_runtime.h>
#include <hip/hip_bf16.h>
#include <stdint.h>

typedef unsigned int u32;
typedef unsigned short u16;
using u32x4 = __attribute__((ext_vector_type(4))) unsigned;
using f32x4 = __attribute__((ext_vector_type(4))) float;
using bf16x2 = __attribute__((ext_vector_type(2))) __bf16;

constexpr int NB = 2, HQ = 16, HKV = 8, SK = 32768, DH = 256, DM = 448, FF = 1792;
constexpr int NQ = HQ * DH;
constexpr int NSPLIT = 16;
constexpr int KEYS_PER_SPLIT = SK / NSPLIT;
constexpr int AW = 8;
constexpr int STAGE = 16;
constexpr float LOG2E = 1.4426950408889634f;

constexpr size_t WS_CNT = 0;
constexpr size_t WS_Q = 4096;
constexpr size_t WS_H = WS_Q + (size_t)NB * NQ * 2;
constexpr size_t WS_PML = 32768;
constexpr size_t WS_PO = 65536;
constexpr size_t WS_SLAB = WS_PO + (size_t)NB * HQ * NSPLIT * DH * 4;

__device__ __forceinline__ float bfr(float x) { return (float)(__bf16)x; }
__device__ __forceinline__ float bf_lo(u32 v) { return __uint_as_float(v << 16); }
__device__ __forceinline__ float bf_hi(u32 v) { return __uint_as_float(v & 0xffff0000u); }
__device__ __forceinline__ float bf2f(u16 v) { return __uint_as_float((u32)v << 16); }
__device__ __forceinline__ u16 f2bf(float x) { __bf16 b = (__bf16)x; return __builtin_bit_cast(u16, b); }
__device__ __forceinline__ float dot2(u32 k, u32 q, float acc) {
  return __builtin_amdgcn_fdot2_f32_bf16(__builtin_bit_cast(bf16x2, k), __builtin_bit_cast(bf16x2, q), acc, false);
}
template <int CTRL> __device__ __forceinline__ float dpp(float x) {
  return __builtin_bit_cast(float, __builtin_amdgcn_mov_dpp(__builtin_bit_cast(int, x), CTRL, 0xf, 0xf, true));
}
constexpr int XOR1 = 0xB1, XOR2 = 0x4E, XOR7 = 0x141, XOR8 = 0x128, ROR4 = 0x124;

__device__ __forceinline__ void ln_row(const u16* __restrict__ xrow, float* xn  , int lane) {
  float v[7];
  float s = 0.f;
#pragma unroll
  for (int i = 0; i < 7; ++i) { v[i] = bf2f(xrow[lane + 64 * i]); s += v[i]; }
#pragma unroll
  for (int d = 32; d > 0; d >>= 1) s += __shfl_xor(s, d, 64);
  const float mean = s * (1.0f / DM);
  float q = 0.f;
#pragma unroll
  for (int i = 0; i < 7; ++i) { const float t = v[i] - mean; q += t * t; }
#pragma unroll
  for (int d = 32; d > 0; d >>= 1) q += __shfl_xor(q, d, 64);
  const float m_bf = bfr(mean);
  const float var_bf = bfr(q * (1.0f / DM));
  const float den = bfr(sqrtf(bfr(var_bf + 1e-5f)));
#pragma unroll
  for (int i = 0; i < 7; ++i) xn[lane + 64 * i] = bfr(bfr(v[i] - m_bf) / den);
}

struct Stg { u32x4 k[8]; u32x4 v[8]; };

__device__ __forceinline__ void stg_issue(Stg& s, const u32x4* __restrict__ kp, const u32x4* __restrict__ vp) {
#pragma unroll
  for (int i = 0; i < 8; ++i) s.k[i] = __builtin_nontemporal_load(kp + i * 64);
#pragma unroll
  for (int i = 0; i < 8; ++i) s.v[i] = __builtin_nontemporal_load(vp + i * 64);
}

__device__ __forceinline__ void stg_compute(const Stg& s, const u32x4 (&q)[2], float (&acc)[2][8], float& m, float& l,
                                            float* prow  , int lane) {
  const int r = lane >> 5, c = lane & 31;
  float x[16];
#pragma unroll
  for (int i = 0; i < 8; ++i)
#pragma unroll
    for (int g = 0; g < 2; ++g) {
      float d = 0.f;
#pragma unroll
      for (int k = 0; k < 4; ++k) d = dot2(s.k[i][k], q[g][k], d);
      x[i * 2 + g] = d;
    }
#pragma unroll
  for (int j = 0; j < 8; ++j) {
    auto t = __builtin_amdgcn_permlane16_swap(__float_as_uint(x[j]), __float_as_uint(x[j + 8]), false, false);
    x[j] = __uint_as_float(t[0]) + __uint_as_float(t[1]);
  }
#define TR_STEP(HALF, CTRL, BIT) _Pragma("unroll") for (int h = 0; h < (HALF); ++h) { \
    const float lo_ = x[h], hi_ = x[h + (HALF)]; const float t1_ = lo_ + dpp<CTRL>(lo_), t2_ = hi_ + dpp<CTRL>(hi_); \
    x[h] = (c & (BIT)) ? t2_ : t1_; }
  TR_STEP(4, XOR8, 8) TR_STEP(2, XOR7, 4) TR_STEP(1, XOR2, 2)
#undef TR_STEP
  float sc = x[0] + dpp<XOR1>(x[0]);
  sc = bfr(sc) * (0.0625f * LOG2E);
  float pm = sc;
  pm = fmaxf(pm, dpp<ROR4>(pm)); pm = fmaxf(pm, dpp<XOR8>(pm));
  {
    auto t = __builtin_amdgcn_permlane16_swap(__float_as_uint(pm), __float_as_uint(pm), false, false);
    pm = fmaxf(__uint_as_float(t[0]), __uint_as_float(t[1]));
    auto u = __builtin_amdgcn_permlane32_swap(__float_as_uint(pm), __float_as_uint(pm), false, false);
    pm = fmaxf(__uint_as_float(u[0]), __uint_as_float(u[1]));
  }
  const float mn = fmaxf(m, pm);
  const float alpha = __builtin_amdgcn_exp2f(m - mn);
  const float p = __builtin_amdgcn_exp2f(sc - mn);
  l = fmaf(l, alpha, p);
  m = mn;
  const float a0 = dpp<0x00>(alpha), a1 = dpp<0xAA>(alpha);
#pragma unroll
  for (int k = 0; k < 8; ++k) { acc[0][k] *= a0; acc[1][k] *= a1; }
  prow[r * 16 + (c >> 1)] = p;
  f32x4 pr[4];
#pragma unroll
  for (int u = 0; u < 4; ++u) pr[u] = *reinterpret_cast<const f32x4*>(prow + r * 16 + 4 * u);
#pragma unroll
  for (int i = 0; i < 8; ++i) {
    const float p0 = pr[i >> 1][(i & 1) * 2], p1 = pr[i >> 1][(i & 1) * 2 + 1];
#pragma unroll
    for (int k = 0; k < 4; ++k) {
      const float lo = bf_lo(s.v[i][k]), hi = bf_hi(s.v[i][k]);
      acc[0][2 * k] = fmaf(p0, lo, acc[0][2 * k]); acc[0][2 * k + 1] = fmaf(p0, hi, acc[0][2 * k + 1]);
      acc[1][2 * k] = fmaf(p1, lo, acc[1][2 * k]); acc[1][2 * k + 1] = fmaf(p1, hi, acc[1][2 * k + 1]);
    }
  }
}

constexpr int NST = KEYS_PER_SPLIT / STAGE;
constexpr int QROWS = DM / 4;
__global__ void __launch_bounds__(AW * 64, 2) k2_attn(const u16* __restrict__ x, const u16* __restrict__ wq,
                                                      const u16* __restrict__ wg, const u16* __restrict__ wu,
                                                      const u16* __restrict__ kc, const u16* __restrict__ vc,
                                                      unsigned char* __restrict__ ws) {
  __shared__ __attribute__((aligned(16))) float lds_p[AW][32];
  __shared__ __attribute__((aligned(16))) float lds_acc[AW][2][DH];
  __shared__ __attribute__((aligned(16))) float lds_xn[4][2][DM];
  __shared__ __attribute__((aligned(16))) float lds_qp[4][2 * DH];
  __shared__ float lds_f[32];
  __shared__ float lds_ml[AW][2][2];
  __shared__ u32 lds_ctr;
  const int tid = threadIdx.x, lane = tid & 63, wave = __builtin_amdgcn_readfirstlane(tid >> 6);
  const int bid = blockIdx.x;
  const int kvh = bid & 7, b = bid >> 7, split = (bid >> 3) & 15;
  const int bh = b * HKV + kvh;
  const int c = lane & 31;
  const size_t base = ((size_t)bh * SK + (size_t)split * KEYS_PER_SPLIT) * DH;
  const u32x4* kp = reinterpret_cast<const u32x4*>(kc + base) + lane;
  const u32x4* vp = reinterpret_cast<const u32x4*>(vc + base) + lane;
  const u32x4* kdum = reinterpret_cast<const u32x4*>(kc);
  const u32x4* vdum = reinterpret_cast<const u32x4*>(vc);
  constexpr int SSTRIDE = STAGE * DH / 8;

  Stg A, Bf;
  int sA, sB;
  if (wave < 4) {
    sA = wave; sB = wave + 4;
    stg_issue(A, kp + (size_t)sA * SSTRIDE, vp + (size_t)sA * SSTRIDE);
    stg_issue(Bf, kp + (size_t)sB * SSTRIDE, vp + (size_t)sB * SSTRIDE);
    if (tid == 0) lds_ctr = 8u;
    if (bid == 0 && tid < 8) reinterpret_cast<u32*>(ws + WS_CNT)[tid * 16] = 0u;
  } else {
    const int pw = wave - 4;
    float* xn = &lds_xn[pw][0][0];
    ln_row(x + b * DM, xn, lane);
    const u32x4* wp = reinterpret_cast<const u32x4*>(wq + (size_t)(pw * QROWS) * NQ + kvh * 2 * DH) + lane;
    float a8[8];
#pragma unroll
    for (int j = 0; j < 8; ++j) a8[j] = 0.f;
#pragma unroll 1
    for (int r0 = 0; r0 < QROWS; r0 += 28) {
      u32x4 w[28];
#pragma unroll
      for (int i = 0; i < 28; ++i) w[i] = wp[(size_t)(r0 + i) * (NQ / 8)];
#pragma unroll
      for (int i4 = 0; i4 < 7; ++i4) {
        const f32x4 xv = *reinterpret_cast<const f32x4*>(xn + pw * QROWS + r0 + 4 * i4);
#pragma unroll
        for (int ii = 0; ii < 4; ++ii) {
#pragma unroll
          for (int j = 0; j < 4; ++j) {
            a8[2 * j] = fmaf(xv[ii], bf_lo(w[4 * i4 + ii][j]), a8[2 * j]);
            a8[2 * j + 1] = fmaf(xv[ii], bf_hi(w[4 * i4 + ii][j]), a8[2 * j + 1]);
          }
        }
      }
    }
    *reinterpret_cast<f32x4*>(&lds_qp[pw][8 * lane]) = f32x4{a8[0], a8[1], a8[2], a8[3]};
    *reinterpret_cast<f32x4*>(&lds_qp[pw][8 * lane + 4]) = f32x4{a8[4], a8[5], a8[6], a8[7]};
  }
  __syncthreads();
  u32x4 q[2];
#pragma unroll
  for (int g = 0; g < 2; ++g) {
    f32x4 lo = *reinterpret_cast<const f32x4*>(&lds_qp[0][g * DH + 8 * c]);
    f32x4 hi = *reinterpret_cast<const f32x4*>(&lds_qp[0][g * DH + 8 * c + 4]);
#pragma unroll
    for (int pw = 1; pw < 4; ++pw) {
      lo += *reinterpret_cast<const f32x4*>(&lds_qp[pw][g * DH + 8 * c]);
      hi += *reinterpret_cast<const f32x4*>(&lds_qp[pw][g * DH + 8 * c + 4]);
    }
    q[g][0] = (u32)f2bf(lo[0]) | ((u32)f2bf(lo[1]) << 16);
    q[g][1] = (u32)f2bf(lo[2]) | ((u32)f2bf(lo[3]) << 16);
    q[g][2] = (u32)f2bf(hi[0]) | ((u32)f2bf(hi[1]) << 16);
    q[g][3] = (u32)f2bf(hi[2]) | ((u32)f2bf(hi[3]) << 16);
  }
#define CLAIM() __builtin_amdgcn_readfirstlane((lane == 0) ? (int)atomicAdd(&lds_ctr, 1u) : 0)
#define ISSUE(S, s_) do { const bool ok_ = (s_) < NST; \
    stg_issue(S, ok_ ? kp + (size_t)(s_) * SSTRIDE : kdum, ok_ ? vp + (size_t)(s_) * SSTRIDE : vdum); } while (0)
  if (wave >= 4) {
    if (wave == 7 && (bid >> 3) < 28) {
      float* xn = &lds_xn[3][0][0];
      ln_row(x + (1 - b) * DM, xn + DM, lane);
      const int col0 = ((bid & 7) * 28 + (bid >> 3)) * 8;
      const u16* wsrc = ((lane >> 5) ? wu : wg) + col0;
      const int rl = lane & 31;
      u32x4 w[14];
#pragma unroll
      for (int i = 0; i < 14; ++i) w[i] = *reinterpret_cast<const u32x4*>(wsrc + (size_t)(rl + 32 * i) * FF);
      float f[16];
#pragma unroll
      for (int j = 0; j < 16; ++j) f[j] = 0.f;
#pragma unroll
      for (int i = 0; i < 14; ++i) {
        const float x0 = xn[rl + 32 * i], x1 = xn[DM + rl + 32 * i];
#pragma unroll
        for (int j = 0; j < 4; ++j) {
          const float lo = bf_lo(w[i][j]), hi = bf_hi(w[i][j]);
          f[2 * j] = fmaf(x0, lo, f[2 * j]); f[2 * j + 1] = fmaf(x0, hi, f[2 * j + 1]);
          f[8 + 2 * j] = fmaf(x1, lo, f[8 + 2 * j]); f[8 + 2 * j + 1] = fmaf(x1, hi, f[8 + 2 * j + 1]);
        }
      }
#pragma unroll
      for (int j = 0; j < 16; ++j) {
        float t = f[j];
#pragma unroll
        for (int d = 16; d > 0; d >>= 1) t += __shfl_xor(t, d, 64);
        f[j] = t;
      }
      if (rl == 0) {
#pragma unroll
        for (int j = 0; j < 16; ++j) lds_f[(lane >> 5) * 16 + j] = f[j];
      }
      if (lane < 16) {
        const float g = bfr(lds_f[lane]), u = bfr(lds_f[16 + lane]);
        const float inner = 0.7978845608028654f * (g + 0.044715f * g * g * g);
        const float ge = bfr(0.5f * g * (1.0f + tanhf(inner)));
        const int row = (lane >> 3) ? (1 - b) : b;
        reinterpret_cast<u16*>(ws + WS_H)[row * FF + col0 + (lane & 7)] = f2bf(ge * u);
      }
    }
    sA = CLAIM(); ISSUE(A, sA);
    sB = CLAIM(); ISSUE(Bf, sB);
  }
  float acc[2][8];
#pragma unroll
  for (int g = 0; g < 2; ++g)
#pragma unroll
    for (int k = 0; k < 8; ++k) acc[g][k] = 0.f;
  float m = -INFINITY, l = 0.f;
  float* prow = &lds_p[wave][0];
#pragma unroll 1
  while (true) {
    if (sA >= NST) break;
    stg_compute(A, q, acc, m, l, prow, lane);
    sA = CLAIM(); ISSUE(A, sA);
    if (sB >= NST) break;
    stg_compute(Bf, q, acc, m, l, prow, lane);
    sB = CLAIM(); ISSUE(Bf, sB);
  }
#undef CLAIM
#undef ISSUE
#pragma unroll
  for (int g = 0; g < 2; ++g)
#pragma unroll
    for (int k = 0; k < 8; ++k) {
      auto u = __builtin_amdgcn_permlane32_swap(__float_as_uint(acc[g][k]), __float_as_uint(acc[g][k]), false, false);
      acc[g][k] = __uint_as_float(u[0]) + __uint_as_float(u[1]);
    }
  {
    float t = l;
    t += dpp<ROR4>(t); t += dpp<XOR8>(t);
    auto s1 = __builtin_amdgcn_permlane16_swap(__float_as_uint(t), __float_as_uint(t), false, false);
    t = __uint_as_float(s1[0]) + __uint_as_float(s1[1]);
    auto s2 = __builtin_amdgcn_permlane32_swap(__float_as_uint(t), __float_as_uint(t), false, false);
    l = __uint_as_float(s2[0]) + __uint_as_float(s2[1]);
  }
  if (lane < 32) {
#pragma unroll
    for (int g = 0; g < 2; ++g) {
      *reinterpret_cast<f32x4*>(&lds_acc[wave][g][8 * c]) = f32x4{acc[g][0], acc[g][1], acc[g][2], acc[g][3]};
      *reinterpret_cast<f32x4*>(&lds_acc[wave][g][8 * c + 4]) = f32x4{acc[g][4], acc[g][5], acc[g][6], acc[g][7]};
    }
    if (c == 0 || c == 2) { lds_ml[wave][c >> 1][0] = m; lds_ml[wave][c >> 1][1] = l; }
  }
  __syncthreads();
  {
    const int g = tid >> 8, d = tid & 255;
    float M = -INFINITY;
#pragma unroll
    for (int w = 0; w < AW; ++w) M = fmaxf(M, lds_ml[w][g][0]);
    float L = 0.f, o = 0.f;
#pragma unroll
    for (int w = 0; w < AW; ++w) {
      const float wt = __builtin_amdgcn_exp2f(lds_ml[w][g][0] - M);
      L = fmaf(wt, lds_ml[w][g][1], L);
      o = fmaf(wt, lds_acc[w][g][d], o);
    }
    const size_t row = ((size_t)b * HQ + kvh * 2 + g) * NSPLIT + split;
    reinterpret_cast<float*>(ws + WS_PO)[row * DH + d] = o;
    if (d == 0) { reinterpret_cast<float*>(ws + WS_PML)[row * 2] = M; reinterpret_cast<float*>(ws + WS_PML)[row * 2 + 1] = L; }
  }
}

constexpr int K3_CG = 7, K3_KC = 23;
__global__ void __launch_bounds__(256) k3_out(const u16* __restrict__ x, const u16* __restrict__ wo,
                                              const u16* __restrict__ wd, unsigned char* __restrict__ ws,
                                              u16* __restrict__ out) {
  __shared__ float act[2][DH];
  __shared__ float red[4][8][16];
  __shared__ u32 s_last;
  const int tid = threadIdx.x, lane = tid & 63, wave = tid >> 6;
  const int cg = blockIdx.x % K3_CG, kcx = blockIdx.x / K3_CG;
  const int cq = tid & 7, rg = tid >> 3;
  const u16* wbase = (kcx < HQ ? wo + (size_t)kcx * DH * DM : wd + (size_t)(kcx - HQ) * DH * DM) + cg * 64 + cq * 8;
  u32x4 w[8];
#pragma unroll
  for (int i = 0; i < 8; ++i) w[i] = *reinterpret_cast<const u32x4*>(wbase + (size_t)(rg + 32 * i) * DM);
  if (kcx < HQ) {
    const float* pml = reinterpret_cast<const float*>(ws + WS_PML);
    const float* po = reinterpret_cast<const float*>(ws + WS_PO);
#pragma unroll
    for (int b = 0; b < 2; ++b) {
      const size_t row0 = ((size_t)b * HQ + kcx) * NSPLIT;
      float a[NSPLIT];
#pragma unroll
      for (int s = 0; s < NSPLIT; ++s) a[s] = po[(row0 + s) * DH + tid];
      float M = -INFINITY;
#pragma unroll
      for (int s = 0; s < NSPLIT; ++s) M = fmaxf(M, pml[(row0 + s) * 2]);
      float L = 0.f, o = 0.f;
#pragma unroll
      for (int s = 0; s < NSPLIT; ++s) {
        const float wt = __builtin_amdgcn_exp2f(pml[(row0 + s) * 2] - M);
        L = fmaf(wt, pml[(row0 + s) * 2 + 1], L);
        o = fmaf(wt, a[s], o);
      }
      act[b][tid] = bfr(o / L);
    }
  } else {
    const u16* h = reinterpret_cast<const u16*>(ws + WS_H);
#pragma unroll
    for (int b = 0; b < 2; ++b) act[b][tid] = bf2f(h[b * FF + (kcx - HQ) * DH + tid]);
  }
  __syncthreads();
  float acc[2][8];
#pragma unroll
  for (int b = 0; b < 2; ++b)
#pragma unroll
    for (int j = 0; j < 8; ++j) acc[b][j] = 0.f;
#pragma unroll
  for (int i = 0; i < 8; ++i) {
    const float x0 = act[0][rg + 32 * i], x1 = act[1][rg + 32 * i];
#pragma unroll
    for (int j = 0; j < 4; ++j) {
      const float lo = bf_lo(w[i][j]), hi = bf_hi(w[i][j]);
      acc[0][2 * j] = fmaf(x0, lo, acc[0][2 * j]); acc[0][2 * j + 1] = fmaf(x0, hi, acc[0][2 * j + 1]);
      acc[1][2 * j] = fmaf(x1, lo, acc[1][2 * j]); acc[1][2 * j + 1] = fmaf(x1, hi, acc[1][2 * j + 1]);
    }
  }
#pragma unroll
  for (int b = 0; b < 2; ++b)
#pragma unroll
    for (int j = 0; j < 8; ++j) {
      float t = acc[b][j];
      t += __shfl_xor(t, 8, 64); t += __shfl_xor(t, 16, 64); t += __shfl_xor(t, 32, 64);
      acc[b][j] = t;
    }
  if (lane < 8) {
#pragma unroll
    for (int b = 0; b < 2; ++b)
#pragma unroll
      for (int j = 0; j < 8; ++j) red[wave][lane][b * 8 + j] = acc[b][j];
  }
  __syncthreads();
  float* slab = reinterpret_cast<float*>(ws + WS_SLAB) + (size_t)cg * K3_KC * 128;
  const int ob = tid >> 6, oc = tid & 63;
  if (tid < 128) {
    const int c8 = oc >> 3, j = oc & 7;
    const float t = (red[0][c8][ob * 8 + j] + red[1][c8][ob * 8 + j]) + (red[2][c8][ob * 8 + j] + red[3][c8][ob * 8 + j]);
    __hip_atomic_store(slab + kcx * 128 + tid, t, __ATOMIC_RELAXED, __HIP_MEMORY_SCOPE_AGENT);
  }
  asm volatile("s_waitcnt vmcnt(0)" ::: "memory");
  __syncthreads();
  if (tid == 0) {
    u32* cnt = reinterpret_cast<u32*>(ws + WS_CNT) + cg * 16;
    const u32 old = __hip_atomic_fetch_add(cnt, 1u, __ATOMIC_RELAXED, __HIP_MEMORY_SCOPE_AGENT);
    s_last = (old == (u32)(K3_KC - 1)) ? 1u : 0u;
  }
  __syncthreads();
  if (s_last == 0u) return;
  if (tid < 128) {
    float v[K3_KC];
#pragma unroll
    for (int k = 0; k < K3_KC; ++k) v[k] = __hip_atomic_load(slab + k * 128 + tid, __ATOMIC_RELAXED, __HIP_MEMORY_SCOPE_AGENT);
    float sa = 0.f, sf = 0.f;
#pragma unroll
    for (int k = 0; k < HQ; ++k) sa += v[k];
#pragma unroll
    for (int k = HQ; k < K3_KC; ++k) sf += v[k];
    const int n = cg * 64 + oc;
    const float xv = bf2f(x[ob * DM + n]);
    out[ob * DM + n] = f2bf(bfr(xv + bfr(sa)) + bfr(sf));
  }
}

extern "C" void kernel_launch(void* const* d_in, const int* in_sizes, int n_in,
                              void* d_out, int out_size, void* d_ws, size_t ws_size,
                              hipStream_t stream) {
  const u16* x = (const u16*)d_in[0];
  const u16* kc = (const u16*)d_in[1];
  const u16* vc = (const u16*)d_in[2];
  const u16* wq = (const u16*)d_in[3];
  const u16* wo = (const u16*)d_in[4];
  const u16* wg = (const u16*)d_in[6];
  const u16* wu = (const u16*)d_in[7];
  const u16* wd = (const u16*)d_in[8];
  unsigned char* ws = (unsigned char*)d_ws;
  k2_attn<<<NB * HKV * NSPLIT, AW * 64, 0, stream>>>(x, wq, wg, wu, kc, vc, ws);
  k3_out<<<K3_CG * K3_KC, 256, 0, stream>>>(x, wo, wd, ws, (u16*)d_out);
}
```

```cpp
#include <hip/hip_runtime.h>
#include <hip/hip_bf16.h>
#include <stdint.h>

typedef unsigned int u32;
typedef unsigned short u16;
using u32x4 = __attribute__((ext_vector_type(4))) unsigned;
using f32x4 = __attribute__((ext_vector_type(4))) float;
using bf16x2 = __attribute__((ext_vector_type(2))) __bf16;

constexpr int NB = 2, HQ = 16, HKV = 8, SK = 32768, DH = 256, DM = 448, FF = 1792;
constexpr int NQ = HQ * DH;
constexpr int NSPLIT = 16;
constexpr int AW = 8;
constexpr int STAGE = 16;
constexpr int NSTG = SK / STAGE;
constexpr int RSTG = NSTG / NSPLIT;
constexpr int OWN = 112;
constexpr int POOL = NSPLIT * (RSTG - OWN);
constexpr float LOG2E = 1.4426950408889634f;

constexpr size_t WS_CNT = 0;
constexpr size_t WS_POOL = 1024;
constexpr size_t WS_Q = 4096;
constexpr size_t WS_H = WS_Q + (size_t)NB * NQ * 2;
constexpr size_t WS_PML = 32768;
constexpr size_t WS_PO = 65536;
constexpr size_t WS_SLAB = WS_PO + (size_t)NB * HQ * NSPLIT * DH * 4;

__device__ __forceinline__ float bfr(float x) { return (float)(__bf16)x; }
__device__ __forceinline__ float bf_lo(u32 v) { return __uint_as_float(v << 16); }
__device__ __forceinline__ float bf_hi(u32 v) { return __uint_as_float(v & 0xffff0000u); }
__device__ __forceinline__ float bf2f(u16 v) { return __uint_as_float((u32)v << 16); }
__device__ __forceinline__ u16 f2bf(float x) { __bf16 b = (__bf16)x; return __builtin_bit_cast(u16, b); }
__device__ __forceinline__ float dot2(u32 k, u32 q, float acc) {
  return __builtin_amdgcn_fdot2_f32_bf16(__builtin_bit_cast(bf16x2, k), __builtin_bit_cast(bf16x2, q), acc, false);
}
template <int CTRL> __device__ __forceinline__ float dpp(float x) {
  return __builtin_bit_cast(float, __builtin_amdgcn_mov_dpp(__builtin_bit_cast(int, x), CTRL, 0xf, 0xf, true));
}
constexpr int XOR1 = 0xB1, XOR2 = 0x4E, XOR7 = 0x141, XOR8 = 0x128, ROR4 = 0x124;
__device__ __forceinline__ float sum16x(float x) {
  auto t = __builtin_amdgcn_permlane16_swap(__float_as_uint(x), __float_as_uint(x), false, false);
  return __uint_as_float(t[0]) + __uint_as_float(t[1]);
}
__device__ __forceinline__ float sum32x(float x) {
  auto t = __builtin_amdgcn_permlane32_swap(__float_as_uint(x), __float_as_uint(x), false, false);
  return __uint_as_float(t[0]) + __uint_as_float(t[1]);
}
__device__ __forceinline__ float wave_sum(float x) {
  x += dpp<XOR1>(x); x += dpp<XOR2>(x); x += dpp<XOR7>(x); x += dpp<XOR8>(x);
  return sum32x(sum16x(x));
}

constexpr int K1_QWG = NQ / 32, K1_FWG = FF / 16;
__global__ void __launch_bounds__(256) k1_proj(const u16* __restrict__ x, const u16* __restrict__ wq,
                                               const u16* __restrict__ wg, const u16* __restrict__ wu,
                                               unsigned char* __restrict__ ws) {
  __shared__ float xn[NB * DM];
  __shared__ float red[4][4][16];
  const int tid = threadIdx.x, lane = tid & 63, wave = tid >> 6;
  const int wgid = blockIdx.x;
  const int cq = tid & 3, rg = tid >> 2;
  const bool isq = wgid < K1_QWG;
  float v[7];
#pragma unroll
  for (int i = 0; i < 7; ++i) v[i] = bf2f(x[(wave & 1) * DM + lane + 64 * i]);
  const u16* wbase; int ld;
  if (isq) { wbase = wq + wgid * 32 + cq * 8; ld = NQ; }
  else { const int f = wgid - K1_QWG; wbase = ((cq & 2) ? wu : wg) + f * 16 + (cq & 1) * 8; ld = FF; }
  u32x4 w[7];
#pragma unroll
  for (int i = 0; i < 7; ++i) w[i] = *reinterpret_cast<const u32x4*>(wbase + (size_t)(rg + 64 * i) * ld);
  if (wgid == 0) {
    if (tid < 8) reinterpret_cast<u32*>(ws + WS_CNT)[tid * 16] = 0u;
    else if (tid >= 64 && tid < 80) reinterpret_cast<u32*>(ws + WS_POOL)[(tid - 64) * 32] = 0u;
  }
  if (wave < NB) {
    float s = 0.f;
#pragma unroll
    for (int i = 0; i < 7; ++i) s += v[i];
    const float mean = wave_sum(s) * (1.0f / DM);
    float q = 0.f;
#pragma unroll
    for (int i = 0; i < 7; ++i) { const float t = v[i] - mean; q += t * t; }
    const float m_bf = bfr(mean);
    const float var_bf = bfr(wave_sum(q) * (1.0f / DM));
    const float den = bfr(sqrtf(bfr(var_bf + 1e-5f)));
#pragma unroll
    for (int i = 0; i < 7; ++i) xn[wave * DM + lane + 64 * i] = bfr(bfr(v[i] - m_bf) / den);
  }
  __syncthreads();
  float acc[16];
#pragma unroll
  for (int j = 0; j < 16; ++j) acc[j] = 0.f;
#pragma unroll
  for (int i = 0; i < 7; ++i) {
    const float x0 = xn[rg + 64 * i], x1 = xn[DM + rg + 64 * i];
#pragma unroll
    for (int j = 0; j < 4; ++j) {
      const float lo = bf_lo(w[i][j]), hi = bf_hi(w[i][j]);
      acc[2 * j] = fmaf(x0, lo, acc[2 * j]); acc[2 * j + 1] = fmaf(x0, hi, acc[2 * j + 1]);
      acc[8 + 2 * j] = fmaf(x1, lo, acc[8 + 2 * j]); acc[8 + 2 * j + 1] = fmaf(x1, hi, acc[8 + 2 * j + 1]);
    }
  }
#pragma unroll
  for (int j = 0; j < 16; ++j) {
    float t = acc[j];
    t += dpp<ROR4>(t); t += dpp<XOR8>(t);
    acc[j] = sum32x(sum16x(t));
  }
  if (lane < 4) {
#pragma unroll
    for (int j = 0; j < 16; j += 4) *reinterpret_cast<f32x4*>(&red[wave][lane][j]) = f32x4{acc[j], acc[j + 1], acc[j + 2], acc[j + 3]};
  }
  __syncthreads();
  if (tid < 64) {
    const int c4 = tid >> 4, idx = tid & 15;
    const float t = (red[0][c4][idx] + red[1][c4][idx]) + (red[2][c4][idx] + red[3][c4][idx]);
    if (isq) {
      const int b = idx >> 3, j = idx & 7;
      reinterpret_cast<u16*>(ws + WS_Q)[b * NQ + wgid * 32 + c4 * 8 + j] = f2bf(t);
    } else if (tid < 32) {
      const float up = (red[0][c4 + 2][idx] + red[1][c4 + 2][idx]) + (red[2][c4 + 2][idx] + red[3][c4 + 2][idx]);
      const int b = idx >> 3, j = idx & 7;
      const float g = bfr(t), u = bfr(up);
      const float inner = 0.7978845608028654f * (g + 0.044715f * g * g * g);
      const float ge = bfr(0.5f * g * (1.0f + tanhf(inner)));
      reinterpret_cast<u16*>(ws + WS_H)[b * FF + (wgid - K1_QWG) * 16 + c4 * 8 + j] = f2bf(ge * u);
    }
  }
}

#define KV_LOAD8(R, VOFF, SB) asm volatile("s_nop 4\n\t" \
  "global_load_dwordx4 %0, %8, %9 offset:-4096 nt\n\t" \
  "global_load_dwordx4 %1, %8, %9 offset:-3072 nt\n\t" \
  "global_load_dwordx4 %2, %8, %9 offset:-2048 nt\n\t" \
  "global_load_dwordx4 %3, %8, %9 offset:-1024 nt\n\t" \
  "global_load_dwordx4 %4, %8, %9 nt\n\t" \
  "global_load_dwordx4 %5, %8, %9 offset:1024 nt\n\t" \
  "global_load_dwordx4 %6, %8, %9 offset:2048 nt\n\t" \
  "global_load_dwordx4 %7, %8, %9 offset:3072 nt" \
  : "=&v"(R[0]), "=&v"(R[1]), "=&v"(R[2]), "=&v"(R[3]), "=&v"(R[4]), "=&v"(R[5]), "=&v"(R[6]), "=&v"(R[7]) \
  : "v"(VOFF), "s"(SB) : "memory")
#define KV_WAIT8(R, N) asm volatile("s_waitcnt vmcnt(" #N ")" \
  : "+v"(R[0]), "+v"(R[1]), "+v"(R[2]), "+v"(R[3]), "+v"(R[4]), "+v"(R[5]), "+v"(R[6]), "+v"(R[7]) :: "memory")

struct Stg { u32x4 k[8]; u32x4 v[8]; };

__device__ __forceinline__ float stg_qk(const u32x4 (&kk)[8], const u32x4 (&q)[2], int lane) {
  const int c = lane & 31;
  float x[16];
#pragma unroll
  for (int i = 0; i < 8; ++i)
#pragma unroll
    for (int g = 0; g < 2; ++g) {
      float d = 0.f;
#pragma unroll
      for (int k = 0; k < 4; ++k) d = dot2(kk[i][k], q[g][k], d);
      x[i * 2 + g] = d;
    }
#pragma unroll
  for (int j = 0; j < 8; ++j) {
    auto t = __builtin_amdgcn_permlane16_swap(__float_as_uint(x[j]), __float_as_uint(x[j + 8]), false, false);
    x[j] = __uint_as_float(t[0]) + __uint_as_float(t[1]);
  }
#define TR_STEP(HALF, CTRL, BIT) _Pragma("unroll") for (int h = 0; h < (HALF); ++h) { \
    const float lo_ = x[h], hi_ = x[h + (HALF)]; const float t1_ = lo_ + dpp<CTRL>(lo_), t2_ = hi_ + dpp<CTRL>(hi_); \
    x[h] = (c & (BIT)) ? t2_ : t1_; }
  TR_STEP(4, XOR8, 8) TR_STEP(2, XOR7, 4) TR_STEP(1, XOR2, 2)
#undef TR_STEP
  return x[0] + dpp<XOR1>(x[0]);
}
__device__ __forceinline__ void stg_softmax(float sc, float (&acc)[2][8], float& m, float& l, float* prow, int lane, f32x4 (&pr)[4]) {
  const int r = lane >> 5, c = lane & 31;
  sc = bfr(sc) * (0.0625f * LOG2E);
  float pm = sc;
  pm = fmaxf(pm, dpp<ROR4>(pm)); pm = fmaxf(pm, dpp<XOR8>(pm));
  {
    auto t = __builtin_amdgcn_permlane16_swap(__float_as_uint(pm), __float_as_uint(pm), false, false);
    pm = fmaxf(__uint_as_float(t[0]), __uint_as_float(t[1]));
    auto u = __builtin_amdgcn_permlane32_swap(__float_as_uint(pm), __float_as_uint(pm), false, false);
    pm = fmaxf(__uint_as_float(u[0]), __uint_as_float(u[1]));
  }
  const float mn = fmaxf(m, pm);
  const float alpha = __builtin_amdgcn_exp2f(m - mn);
  const float p = __builtin_amdgcn_exp2f(sc - mn);
  l = fmaf(l, alpha, p);
  m = mn;
  const float a0 = dpp<0x00>(alpha), a1 = dpp<0xAA>(alpha);
#pragma unroll
  for (int k = 0; k < 8; ++k) { acc[0][k] *= a0; acc[1][k] *= a1; }
  prow[r * 16 + (c >> 1)] = p;
#pragma unroll
  for (int u = 0; u < 4; ++u) pr[u] = *reinterpret_cast<const f32x4*>(prow + r * 16 + 4 * u);
}
__device__ __forceinline__ void stg_pv(const u32x4 (&vv)[8], const f32x4 (&pr)[4], float (&acc)[2][8]) {
#pragma unroll
  for (int i = 0; i < 8; ++i) {
    const float p0 = pr[i >> 1][(i & 1) * 2], p1 = pr[i >> 1][(i & 1) * 2 + 1];
#pragma unroll
    for (int k = 0; k < 4; ++k) {
      const float lo = bf_lo(vv[i][k]), hi = bf_hi(vv[i][k]);
      acc[0][2 * k] = fmaf(p0, lo, acc[0][2 * k]); acc[0][2 * k + 1] = fmaf(p0, hi, acc[0][2 * k + 1]);
      acc[1][2 * k] = fmaf(p1, lo, acc[1][2 * k]); acc[1][2 * k + 1] = fmaf(p1, hi, acc[1][2 * k + 1]);
    }
  }
}

constexpr int K3_CG = 7, K3_KC = 23;
__global__ void __launch_bounds__(AW * 64, 2) k2_attn(const u16* __restrict__ kc, const u16* __restrict__ vc,
                                                      const u16* __restrict__ wo, const u16* __restrict__ wd,
                                                      unsigned char* __restrict__ ws) {
  __shared__ __attribute__((aligned(16))) float lds_p[AW][32];
  __shared__ __attribute__((aligned(16))) float lds_acc[AW][2][DH];
  __shared__ float lds_ml[AW][2][2];
  __shared__ u32 lds_ctr;
  const int tid = threadIdx.x, lane = tid & 63, wave = __builtin_amdgcn_readfirstlane(tid >> 6);
  const int bid = blockIdx.x;
  const int split = bid & 15, bh = bid >> 4;
  const int b = bh >> 3, kvh = bh & 7;
  const int c = lane & 31;
  const char* kgb = reinterpret_cast<const char*>(kc + (size_t)bh * SK * DH) + 4096;
  const char* vgb = reinterpret_cast<const char*>(vc + (size_t)bh * SK * DH) + 4096;
  const u32 voff = lane * 16;
  u32* gctr = reinterpret_cast<u32*>(ws + WS_POOL) + bh * 32;
  const u32 vzero = 0u, vone = 1u;
  u32 nxt = 0u;
  int pend = 0; bool pg = false;
  Stg A, Bf;
  int sA, sB;
  u32x4 q[2];
#define LCLAIM() __builtin_amdgcn_readfirstlane((lane == 0) ? (int)atomicAdd(&lds_ctr, 1u) : 0)
#define GCLAIM() do { unsigned long long sv_; asm volatile("s_mov_b64 %1, exec\n\ts_mov_b64 exec, 1\n\tglobal_atomic_add %0, %2, %3, %4 sc0\n\ts_mov_b64 exec, %1" \
    : "+v"(nxt), "=&s"(sv_) : "v"(vzero), "v"(vone), "s"(gctr) : "memory"); } while (0)
#define CLAIMNEXT() do { const int l_ = LCLAIM(); if (l_ < OWN) { pend = split * RSTG + l_; pg = false; } else { GCLAIM(); pg = true; } } while (0)
#define TAKE(dst) do { if (pg) { int g_; asm volatile("s_waitcnt vmcnt(16)\n\tv_readfirstlane_b32 %0, %1" : "=s"(g_) : "v"(nxt) : "memory"); \
    dst = (g_ < POOL) ? (g_ & 15) * RSTG + OWN + (g_ >> 4) : NSTG; } else dst = pend; } while (0)
#define ISSUE(S, s_) do { const size_t o_ = ((s_) < NSTG) ? (size_t)(s_) * (STAGE * DH * 2) : (size_t)0;   \
    const char* kb_ = kgb + o_; const char* vb_ = vgb + o_; KV_LOAD8(S.k, voff, kb_); KV_LOAD8(S.v, voff, vb_); } while (0)
  {
    const u16* qp = reinterpret_cast<const u16*>(ws + WS_Q) + b * NQ + (kvh * 2) * DH + 8 * c;
    asm volatile("global_load_dwordx4 %0, %2, off\n\tglobal_load_dwordx4 %1, %2, off offset:512" : "=&v"(q[0]), "=&v"(q[1]) : "v"(qp) : "memory");
  }
  sA = split * RSTG + wave; sB = sA + AW;
  ISSUE(A, sA);
  ISSUE(Bf, sB);
  if (tid == 0) lds_ctr = 2 * AW;
  __syncthreads();
  CLAIMNEXT();
  asm volatile("s_waitcnt vmcnt(32)" : "+v"(q[0]), "+v"(q[1]) :: "memory");
  float acc[2][8];
#pragma unroll
  for (int g = 0; g < 2; ++g)
#pragma unroll
    for (int k = 0; k < 8; ++k) acc[g][k] = 0.f;
  float m = -INFINITY, l = 0.f;
  float* prow = &lds_p[wave][0];
#define HALF(S, s_) \
    if (s_ >= NSTG) break; \
    { KV_WAIT8(S.k, 24); const float sc_ = stg_qk(S.k, q, lane); f32x4 pr_[4]; stg_softmax(sc_, acc, m, l, prow, lane, pr_); \
      KV_WAIT8(S.v, 16); stg_pv(S.v, pr_, acc); } \
    TAKE(s_); CLAIMNEXT(); ISSUE(S, s_);
#pragma unroll 1
  while (true) {
    HALF(A, sA)
    HALF(Bf, sB)
  }
#undef HALF
  KV_WAIT8(A.k, 0); KV_WAIT8(A.v, 0); KV_WAIT8(Bf.k, 0); KV_WAIT8(Bf.v, 0);
  asm volatile("" : "+v"(nxt) :: "memory");
  u32 warm = 0u;
  if (bid < K3_CG * K3_KC) {
    const int cg = bid % K3_CG, kcx = bid / K3_CG;
    const u16* wbase = (kcx < HQ ? wo + (size_t)kcx * DH * DM : wd + (size_t)(kcx - HQ) * DH * DM) + cg * 64 + (tid & 1) * 32;
    warm = *reinterpret_cast<const u32*>(wbase + (size_t)(tid >> 1) * DM);
  }
#pragma unroll
  for (int g = 0; g < 2; ++g)
#pragma unroll
    for (int k = 0; k < 8; ++k) acc[g][k] = sum32x(acc[g][k]);
  {
    float t = l;
    t += dpp<ROR4>(t); t += dpp<XOR8>(t);
    l = sum32x(sum16x(t));
  }
  if (lane < 32) {
#pragma unroll
    for (int g = 0; g < 2; ++g) {
      *reinterpret_cast<f32x4*>(&lds_acc[wave][g][8 * c]) = f32x4{acc[g][0], acc[g][1], acc[g][2], acc[g][3]};
      *reinterpret_cast<f32x4*>(&lds_acc[wave][g][8 * c + 4]) = f32x4{acc[g][4], acc[g][5], acc[g][6], acc[g][7]};
    }
    if (c == 0 || c == 2) { lds_ml[wave][c >> 1][0] = m; lds_ml[wave][c >> 1][1] = l; }
  }
  __syncthreads();
  {
    const int g = tid >> 8, d = tid & 255;
    float M = -INFINITY;
#pragma unroll
    for (int w = 0; w < AW; ++w) M = fmaxf(M, lds_ml[w][g][0]);
    float L = 0.f, o = 0.f;
#pragma unroll
    for (int w = 0; w < AW; ++w) {
      const float wt = __builtin_amdgcn_exp2f(lds_ml[w][g][0] - M);
      L = fmaf(wt, lds_ml[w][g][1], L);
      o = fmaf(wt, lds_acc[w][g][d], o);
    }
    const size_t row = ((size_t)b * HQ + kvh * 2 + g) * NSPLIT + split;
    reinterpret_cast<float*>(ws + WS_PO)[row * DH + d] = o;
    if (d == 0) { reinterpret_cast<float*>(ws + WS_PML)[row * 2] = M; reinterpret_cast<float*>(ws + WS_PML)[row * 2 + 1] = L; }
  }
  asm volatile("" :: "v"(warm));
}

__global__ void __launch_bounds__(256) k3_out(const u16* __restrict__ x, const u16* __restrict__ wo,
                                              const u16* __restrict__ wd, unsigned char* __restrict__ ws,
                                              u16* __restrict__ out) {
  __shared__ float act[2][DH];
  __shared__ float red[4][8][16];
  __shared__ u32 s_last;
  const int tid = threadIdx.x, lane = tid & 63, wave = tid >> 6;
  const int cg = blockIdx.x % K3_CG, kcx = blockIdx.x / K3_CG;
  const int cq = tid & 7, rg = tid >> 3;
  const u16* wbase = (kcx < HQ ? wo + (size_t)kcx * DH * DM : wd + (size_t)(kcx - HQ) * DH * DM) + cg * 64 + cq * 8;
  if (kcx < HQ) {
    const float* pml = reinterpret_cast<const float*>(ws + WS_PML);
    const float* po = reinterpret_cast<const float*>(ws + WS_PO);
    float a[2][NSPLIT];
#pragma unroll
    for (int b = 0; b < 2; ++b)
#pragma unroll
      for (int s = 0; s < NSPLIT; ++s) a[b][s] = po[(((size_t)b * HQ + kcx) * NSPLIT + s) * DH + tid];
    float o2[2];
#pragma unroll
    for (int b = 0; b < 2; ++b) {
      const size_t row0 = ((size_t)b * HQ + kcx) * NSPLIT;
      float M = -INFINITY;
#pragma unroll
      for (int s = 0; s < NSPLIT; ++s) M = fmaxf(M, pml[(row0 + s) * 2]);
      float L = 0.f, o = 0.f;
#pragma unroll
      for (int s = 0; s < NSPLIT; ++s) {
        const float wt = __builtin_amdgcn_exp2f(pml[(row0 + s) * 2] - M);
        L = fmaf(wt, pml[(row0 + s) * 2 + 1], L);
        o = fmaf(wt, a[b][s], o);
      }
      o2[b] = bfr(o / L);
    }
    act[0][tid] = o2[0]; act[1][tid] = o2[1];
  } else {
    const u16* h = reinterpret_cast<const u16*>(ws + WS_H);
#pragma unroll
    for (int b = 0; b < 2; ++b) act[b][tid] = bf2f(h[b * FF + (kcx - HQ) * DH + tid]);
  }
  u32x4 w[8];
#pragma unroll
  for (int i = 0; i < 8; ++i) w[i] = *reinterpret_cast<const u32x4*>(wbase + (size_t)(rg + 32 * i) * DM);
  __syncthreads();
  float acc[16];
#pragma unroll
  for (int j = 0; j < 16; ++j) acc[j] = 0.f;
#pragma unroll
  for (int i = 0; i < 8; ++i) {
    const float x0 = act[0][rg + 32 * i], x1 = act[1][rg + 32 * i];
#pragma unroll
    for (int j = 0; j < 4; ++j) {
      const float lo = bf_lo(w[i][j]), hi = bf_hi(w[i][j]);
      acc[2 * j] = fmaf(x0, lo, acc[2 * j]); acc[2 * j + 1] = fmaf(x0, hi, acc[2 * j + 1]);
      acc[8 + 2 * j] = fmaf(x1, lo, acc[8 + 2 * j]); acc[8 + 2 * j + 1] = fmaf(x1, hi, acc[8 + 2 * j + 1]);
    }
  }
#pragma unroll
  for (int j = 0; j < 16; ++j) {
    float t = acc[j];
    t += dpp<XOR8>(t);
    acc[j] = sum32x(sum16x(t));
  }
  if (lane < 8) {
#pragma unroll
    for (int j = 0; j < 16; j += 4) *reinterpret_cast<f32x4*>(&red[wave][lane][j]) = f32x4{acc[j], acc[j + 1], acc[j + 2], acc[j + 3]};
  }
  __syncthreads();
  float* slab = reinterpret_cast<float*>(ws + WS_SLAB) + (size_t)cg * K3_KC * 128;
  const int ob = tid >> 6, oc = tid & 63;
  if (tid < 128) {
    const int c8 = oc >> 3, j = oc & 7;
    const float t = (red[0][c8][ob * 8 + j] + red[1][c8][ob * 8 + j]) + (red[2][c8][ob * 8 + j] + red[3][c8][ob * 8 + j]);
    __hip_atomic_store(slab + kcx * 128 + tid, t, __ATOMIC_RELAXED, __HIP_MEMORY_SCOPE_AGENT);
  }
  asm volatile("s_waitcnt vmcnt(0)" ::: "memory");
  __syncthreads();
  if (tid == 0) {
    u32* cnt = reinterpret_cast<u32*>(ws + WS_CNT) + cg * 16;
    const u32 old = __hip_atomic_fetch_add(cnt, 1u, __ATOMIC_RELAXED, __HIP_MEMORY_SCOPE_AGENT);
    s_last = (old == (u32)(K3_KC - 1)) ? 1u : 0u;
  }
  __syncthreads();
  if (s_last == 0u) return;
  if (tid < 128) {
    float v[K3_KC];
#pragma unroll
    for (int k = 0; k < K3_KC; ++k) v[k] = __hip_atomic_load(slab + k * 128 + tid, __ATOMIC_RELAXED, __HIP_MEMORY_SCOPE_AGENT);
    float sa = 0.f, sf = 0.f;
#pragma unroll
    for (int k = 0; k < HQ; ++k) sa += v[k];
#pragma unroll
    for (int k = HQ; k < K3_KC; ++k) sf += v[k];
    const int n = cg * 64 + oc;
    const float xv = bf2f(x[ob * DM + n]);
    out[ob * DM + n] = f2bf(bfr(xv + bfr(sa)) + bfr(sf));
  }
}

extern "C" void kernel_launch(void* const* d_in, const int* in_sizes, int n_in,
                              void* d_out, int out_size, void* d_ws, size_t ws_size,
                              hipStream_t stream) {
  const u16* x = (const u16*)d_in[0];
  const u16* kc = (const u16*)d_in[1];
  const u16* vc = (const u16*)d_in[2];
  const u16* wq = (const u16*)d_in[3];
  const u16* wo = (const u16*)d_in[4];
  const u16* wg = (const u16*)d_in[6];
  const u16* wu = (const u16*)d_in[7];
  const u16* wd = (const u16*)d_in[8];
  unsigned char* ws = (unsigned char*)d_ws;
  k1_proj<<<K1_QWG + K1_FWG, 256, 0, stream>>>(x, wq, wg, wu, ws);
  k2_attn<<<NB * HKV * NSPLIT, AW * 64, 0, stream>>>(kc, vc, wo, wd, ws);
  k3_out<<<K3_CG * K3_KC, 256, 0, stream>>>(x, wo, wd, ws, (u16*)d_out);
}
```

```cpp
#include <hip/hip_runtime.h>
#include <hip/hip_bf16.h>
#include <stdint.h>

typedef unsigned int u32;
typedef unsigned short u16;
using u32x4 = __attribute__((ext_vector_type(4))) unsigned;
using f32x4 = __attribute__((ext_vector_type(4))) float;
using bf16x2 = __attribute__((ext_vector_type(2))) __bf16;

constexpr int NB = 2, HQ = 16, HKV = 8, SK = 32768, DH = 256, DM = 448, FF = 1792;
constexpr int NQ = HQ * DH;
constexpr int NSPLIT = 16;
constexpr int AW = 8;
#define KPS 8
#define DEPTH 1
#define OWNFRAC 7
constexpr int STAGE = KPS;
constexpr int NL = KPS / 2;
constexpr int NSTG = SK / STAGE;
constexpr int RSTG = NSTG / NSPLIT;
constexpr int OWN = RSTG * OWNFRAC / 8;
constexpr int POOL = NSPLIT * (RSTG - OWN);
constexpr float LOG2E = 1.4426950408889634f;

constexpr size_t WS_CNT = 0;
constexpr size_t WS_POOL = 1024;
constexpr size_t WS_Q = 4096;
constexpr size_t WS_H = WS_Q + (size_t)NB * NQ * 2;
constexpr size_t WS_PML = 32768;
constexpr size_t WS_PO = 65536;
constexpr size_t WS_SLAB = WS_PO + (size_t)NB * HQ * NSPLIT * DH * 4;

__device__ __forceinline__ float bfr(float x) { return (float)(__bf16)x; }
__device__ __forceinline__ float bf_lo(u32 v) { return __uint_as_float(v << 16); }
__device__ __forceinline__ float bf_hi(u32 v) { return __uint_as_float(v & 0xffff0000u); }
__device__ __forceinline__ float bf2f(u16 v) { return __uint_as_float((u32)v << 16); }
__device__ __forceinline__ u16 f2bf(float x) { __bf16 b = (__bf16)x; return __builtin_bit_cast(u16, b); }
__device__ __forceinline__ float dot2(u32 k, u32 q, float acc) {
  return __builtin_amdgcn_fdot2_f32_bf16(__builtin_bit_cast(bf16x2, k), __builtin_bit_cast(bf16x2, q), acc, false);
}
template <int CTRL> __device__ __forceinline__ float dpp(float x) {
  return __builtin_bit_cast(float, __builtin_amdgcn_mov_dpp(__builtin_bit_cast(int, x), CTRL, 0xf, 0xf, true));
}
constexpr int XOR1 = 0xB1, XOR2 = 0x4E, XOR7 = 0x141, XOR8 = 0x128, ROR4 = 0x124;
__device__ __forceinline__ float sum16x(float x) {
  auto t = __builtin_amdgcn_permlane16_swap(__float_as_uint(x), __float_as_uint(x), false, false);
  return __uint_as_float(t[0]) + __uint_as_float(t[1]);
}
__device__ __forceinline__ float sum32x(float x) {
  auto t = __builtin_amdgcn_permlane32_swap(__float_as_uint(x), __float_as_uint(x), false, false);
  return __uint_as_float(t[0]) + __uint_as_float(t[1]);
}
__device__ __forceinline__ float wave_sum(float x) {
  x += dpp<XOR1>(x); x += dpp<XOR2>(x); x += dpp<XOR7>(x); x += dpp<XOR8>(x);
  return sum32x(sum16x(x));
}

constexpr int K1_QWG = NQ / 32, K1_FWG = FF / 16;
__global__ void __launch_bounds__(256) k1_proj(const u16* __restrict__ x, const u16* __restrict__ wq,
                                               const u16* __restrict__ wg, const u16* __restrict__ wu,
                                               unsigned char* __restrict__ ws) {
  __shared__ float xn[NB * DM];
  __shared__ float red[4][4][16];
  const int tid = threadIdx.x, lane = tid & 63, wave = tid >> 6;
  const int wgid = blockIdx.x;
  const int cq = tid & 3, rg = tid >> 2;
  const bool isq = wgid < K1_QWG;
  float v[7];
#pragma unroll
  for (int i = 0; i < 7; ++i) v[i] = bf2f(x[(wave & 1) * DM + lane + 64 * i]);
  const u16* wbase; int ld;
  if (isq) { wbase = wq + wgid * 32 + cq * 8; ld = NQ; }
  else { const int f = wgid - K1_QWG; wbase = ((cq & 2) ? wu : wg) + f * 16 + (cq & 1) * 8; ld = FF; }
  u32x4 w[7];
#pragma unroll
  for (int i = 0; i < 7; ++i) w[i] = *reinterpret_cast<const u32x4*>(wbase + (size_t)(rg + 64 * i) * ld);
  if (wgid == 0) {
    if (tid < 8) reinterpret_cast<u32*>(ws + WS_CNT)[tid * 16] = 0u;
    else if (tid >= 64 && tid < 80) reinterpret_cast<u32*>(ws + WS_POOL)[(tid - 64) * 32] = 0u;
  }
  if (wave < NB) {
    float s = 0.f;
#pragma unroll
    for (int i = 0; i < 7; ++i) s += v[i];
    const float mean = wave_sum(s) * (1.0f / DM);
    float q = 0.f;
#pragma unroll
    for (int i = 0; i < 7; ++i) { const float t = v[i] - mean; q += t * t; }
    const float m_bf = bfr(mean);
    const float var_bf = bfr(wave_sum(q) * (1.0f / DM));
    const float den = bfr(sqrtf(bfr(var_bf + 1e-5f)));
#pragma unroll
    for (int i = 0; i < 7; ++i) xn[wave * DM + lane + 64 * i] = bfr(bfr(v[i] - m_bf) / den);
  }
  __syncthreads();
  float acc[16];
#pragma unroll
  for (int j = 0; j < 16; ++j) acc[j] = 0.f;
#pragma unroll
  for (int i = 0; i < 7; ++i) {
    const float x0 = xn[rg + 64 * i], x1 = xn[DM + rg + 64 * i];
#pragma unroll
    for (int j = 0; j < 4; ++j) {
      const float lo = bf_lo(w[i][j]), hi = bf_hi(w[i][j]);
      acc[2 * j] = fmaf(x0, lo, acc[2 * j]); acc[2 * j + 1] = fmaf(x0, hi, acc[2 * j + 1]);
      acc[8 + 2 * j] = fmaf(x1, lo, acc[8 + 2 * j]); acc[8 + 2 * j + 1] = fmaf(x1, hi, acc[8 + 2 * j + 1]);
    }
  }
#pragma unroll
  for (int j = 0; j < 16; ++j) {
    float t = acc[j];
    t += dpp<ROR4>(t); t += dpp<XOR8>(t);
    acc[j] = sum32x(sum16x(t));
  }
  if (lane < 4) {
#pragma unroll
    for (int j = 0; j < 16; j += 4) *reinterpret_cast<f32x4*>(&red[wave][lane][j]) = f32x4{acc[j], acc[j + 1], acc[j + 2], acc[j + 3]};
  }
  __syncthreads();
  if (tid < 64) {
    const int c4 = tid >> 4, idx = tid & 15;
    const float t = (red[0][c4][idx] + red[1][c4][idx]) + (red[2][c4][idx] + red[3][c4][idx]);
    if (isq) {
      const int b = idx >> 3, j = idx & 7;
      reinterpret_cast<u16*>(ws + WS_Q)[b * NQ + wgid * 32 + c4 * 8 + j] = f2bf(t);
    } else if (tid < 32) {
      const float up = (red[0][c4 + 2][idx] + red[1][c4 + 2][idx]) + (red[2][c4 + 2][idx] + red[3][c4 + 2][idx]);
      const int b = idx >> 3, j = idx & 7;
      const float g = bfr(t), u = bfr(up);
      const float inner = 0.7978845608028654f * (g + 0.044715f * g * g * g);
      const float ge = bfr(0.5f * g * (1.0f + tanhf(inner)));
      reinterpret_cast<u16*>(ws + WS_H)[b * FF + (wgid - K1_QWG) * 16 + c4 * 8 + j] = f2bf(ge * u);
    }
  }
}

#define KV_LOAD(R, VOFF, SB) asm volatile("s_nop 4\n\t" \
  "global_load_dwordx4 %0, %4, %5 offset:-4096 nt\n\t" \
  "global_load_dwordx4 %1, %4, %5 offset:-3072 nt\n\t" \
  "global_load_dwordx4 %2, %4, %5 offset:-2048 nt\n\t" \
  "global_load_dwordx4 %3, %4, %5 offset:-1024 nt" \
  : "=&v"(R[0]), "=&v"(R[1]), "=&v"(R[2]), "=&v"(R[3]) : "v"(VOFF), "s"(SB) : "memory")
#define KV_WAIT(R, N) asm volatile("s_waitcnt vmcnt(%c4)" : "+v"(R[0]), "+v"(R[1]), "+v"(R[2]), "+v"(R[3]) : "i"(N) : "memory")

struct Stg { u32x4 k[NL]; u32x4 v[NL]; };

__device__ __forceinline__ float stg_qk(const u32x4 (&kk)[NL], const u32x4 (&q)[2], int lane) {
  const int c = lane & 31;
  float x[2 * NL];
#pragma unroll
  for (int i = 0; i < NL; ++i)
#pragma unroll
    for (int g = 0; g < 2; ++g) {
      float d = 0.f;
#pragma unroll
      for (int k = 0; k < 4; ++k) d = dot2(kk[i][k], q[g][k], d);
      x[i * 2 + g] = d;
    }
#pragma unroll
  for (int j = 0; j < NL; ++j) {
    auto t = __builtin_amdgcn_permlane16_swap(__float_as_uint(x[j]), __float_as_uint(x[j + NL]), false, false);
    x[j] = __uint_as_float(t[0]) + __uint_as_float(t[1]);
  }
#define TR_STEP(HALF, CTRL, BIT) _Pragma("unroll") for (int h = 0; h < (HALF); ++h) { \
    const float lo_ = x[h], hi_ = x[h + (HALF)]; const float t1_ = lo_ + dpp<CTRL>(lo_), t2_ = hi_ + dpp<CTRL>(hi_); \
    x[h] = (c & (BIT)) ? t2_ : t1_; }
  TR_STEP(2, XOR8, 8) TR_STEP(1, XOR7, 4)
  float t = x[0] + dpp<XOR2>(x[0]);
  return t + dpp<XOR1>(t);
#undef TR_STEP
}
__device__ __forceinline__ void stg_softmax(float sc, float (&acc)[2][8], float& m, float& l, float* prow, int lane, f32x4 (&pr)[NL / 2]) {
  const int r = lane >> 5, c = lane & 31;
  sc = bfr(sc) * (0.0625f * LOG2E);
  float pm = sc;
  pm = fmaxf(pm, dpp<XOR8>(pm));
  {
    auto t = __builtin_amdgcn_permlane16_swap(__float_as_uint(pm), __float_as_uint(pm), false, false);
    pm = fmaxf(__uint_as_float(t[0]), __uint_as_float(t[1]));
    auto u = __builtin_amdgcn_permlane32_swap(__float_as_uint(pm), __float_as_uint(pm), false, false);
    pm = fmaxf(__uint_as_float(u[0]), __uint_as_float(u[1]));
  }
  const float mn = fmaxf(m, pm);
  const float alpha = __builtin_amdgcn_exp2f(m - mn);
  const float p = __builtin_amdgcn_exp2f(sc - mn);
  l = fmaf(l, alpha, p);
  m = mn;
  const float a0 = __builtin_bit_cast(float, __builtin_amdgcn_readlane(__builtin_bit_cast(int, alpha), 0));
  const float a1 = __builtin_bit_cast(float, __builtin_amdgcn_readlane(__builtin_bit_cast(int, alpha), 4));
  prow[r * 8 + (c >> 2)] = p;
#pragma unroll
  for (int k = 0; k < 8; ++k) { acc[0][k] *= a0; acc[1][k] *= a1; }
#pragma unroll
  for (int u = 0; u < NL / 2; ++u) pr[u] = *reinterpret_cast<const f32x4*>(prow + r * KPS + 4 * u);
}
__device__ __forceinline__ void stg_pv(const u32x4 (&vv)[NL], const f32x4 (&pr)[NL / 2], float (&acc)[2][8]) {
#pragma unroll
  for (int i = 0; i < NL; ++i) {
    const float p0 = pr[i >> 1][(i & 1) * 2], p1 = pr[i >> 1][(i & 1) * 2 + 1];
#pragma unroll
    for (int k = 0; k < 4; ++k) {
      const float lo = bf_lo(vv[i][k]), hi = bf_hi(vv[i][k]);
      acc[0][2 * k] = fmaf(p0, lo, acc[0][2 * k]); acc[0][2 * k + 1] = fmaf(p0, hi, acc[0][2 * k + 1]);
      acc[1][2 * k] = fmaf(p1, lo, acc[1][2 * k]); acc[1][2 * k + 1] = fmaf(p1, hi, acc[1][2 * k + 1]);
    }
  }
}

constexpr int K3_CG = 7, K3_KC = 23;
__global__ void __launch_bounds__(AW * 64, 2) k2_attn(const u16* __restrict__ kc, const u16* __restrict__ vc,
                                                      const u16* __restrict__ wo, const u16* __restrict__ wd,
                                                      unsigned char* __restrict__ ws) {
  __shared__ __attribute__((aligned(16))) float lds_p[AW][32];
  __shared__ __attribute__((aligned(16))) float lds_acc[AW][2][DH];
  __shared__ float lds_ml[AW][2][2];
  __shared__ u32 lds_ctr;
  const int tid = threadIdx.x, lane = tid & 63, wave = __builtin_amdgcn_readfirstlane(tid >> 6);
  const int bid = blockIdx.x;
  const int split = bid & 15, bh = bid >> 4;
  const int b = bh >> 3, kvh = bh & 7;
  const int c = lane & 31;
  const char* kgb = reinterpret_cast<const char*>(kc + (size_t)bh * SK * DH) + 4096;
  const char* vgb = reinterpret_cast<const char*>(vc + (size_t)bh * SK * DH) + 4096;
  const u32 voff = lane * 16;
  u32* gctr = reinterpret_cast<u32*>(ws + WS_POOL) + bh * 32;
  const u32 vzero = 0u, vone = 1u;
  u32 nxt = 0u;
  int pend = 0; bool pg = false;
  Stg A, Bf;
  int sA, sB;
  u32x4 q[2];
#define LCLAIM() __builtin_amdgcn_readfirstlane((lane == 0) ? (int)atomicAdd(&lds_ctr, 1u) : 0)
#define GCLAIM() do { unsigned long long sv_; asm volatile("s_mov_b64 %1, exec\n\ts_mov_b64 exec, 1\n\tglobal_atomic_add %0, %2, %3, %4 sc0\n\ts_mov_b64 exec, %1" \
    : "+v"(nxt), "=&s"(sv_) : "v"(vzero), "v"(vone), "s"(gctr) : "memory"); } while (0)
#define CLAIMNEXT() do { const int l_ = LCLAIM(); if (l_ < OWN) { pend = split * RSTG + l_; pg = false; } else { GCLAIM(); pg = true; } } while (0)
#define TAKE(dst, NW) do { if (pg) { int g_; asm volatile("s_waitcnt vmcnt(%c2)\n\tv_readfirstlane_b32 %0, %1" : "=s"(g_) : "v"(nxt), "i"(NW) : "memory"); \
    dst = (g_ < POOL) ? (g_ & 15) * RSTG + OWN + (g_ >> 4) : NSTG; } else dst = pend; } while (0)
#define SOFF(s_) (((s_) < NSTG) ? (size_t)(s_) * (STAGE * DH * 2) : (size_t)0)
#define ISSUE_K(S, s_) do { const char* kb_ = kgb + SOFF(s_); KV_LOAD(S.k, voff, kb_); } while (0)
#define ISSUE_V(S, s_) do { const char* vb_ = vgb + SOFF(s_); KV_LOAD(S.v, voff, vb_); } while (0)
  {
    const u16* qp = reinterpret_cast<const u16*>(ws + WS_Q) + b * NQ + (kvh * 2) * DH + 8 * c;
    asm volatile("global_load_dwordx4 %0, %2, off\n\tglobal_load_dwordx4 %1, %2, off offset:512" : "=&v"(q[0]), "=&v"(q[1]) : "v"(qp) : "memory");
  }
  float acc[2][8];
#pragma unroll
  for (int g = 0; g < 2; ++g)
#pragma unroll
    for (int k = 0; k < 8; ++k) acc[g][k] = 0.f;
  float m = -INFINITY, l = 0.f;
  float* prow = &lds_p[wave][0];
  sA = split * RSTG + wave;
  ISSUE_K(A, sA); ISSUE_V(A, sA);
  if (tid == 0) lds_ctr = AW;
  __syncthreads();
  CLAIMNEXT();
  asm volatile("s_waitcnt vmcnt(%c2)" : "+v"(q[0]), "+v"(q[1]) : "i"(2 * NL) : "memory");
#pragma unroll 1
  while (sA < NSTG) {
    KV_WAIT(A.k, NL);
    const float sc_ = stg_qk(A.k, q, lane);
    TAKE(sB, NL); CLAIMNEXT(); ISSUE_K(A, sB);
    f32x4 pr_[NL / 2]; stg_softmax(sc_, acc, m, l, prow, lane, pr_);
    KV_WAIT(A.v, NL);
    stg_pv(A.v, pr_, acc);
    ISSUE_V(A, sB);
    sA = sB;
  }
  KV_WAIT(A.k, 0); KV_WAIT(A.v, 0);
  asm volatile("" : "+v"(nxt) :: "memory");
  u32 warm = 0u;
  if (bid < K3_CG * K3_KC) {
    const int cg = bid % K3_CG, kcx = bid / K3_CG;
    const u16* wbase = (kcx < HQ ? wo + (size_t)kcx * DH * DM : wd + (size_t)(kcx - HQ) * DH * DM) + cg * 64 + (tid & 1) * 32;
    warm = *reinterpret_cast<const u32*>(wbase + (size_t)(tid >> 1) * DM);
  }
#pragma unroll
  for (int g = 0; g < 2; ++g)
#pragma unroll
    for (int k = 0; k < 8; ++k) acc[g][k] = sum32x(acc[g][k]);
  {
    float t = l;
    t += dpp<XOR8>(t);
    l = sum32x(sum16x(t));
  }
  if (lane < 32) {
#pragma unroll
    for (int g = 0; g < 2; ++g) {
      *reinterpret_cast<f32x4*>(&lds_acc[wave][g][8 * c]) = f32x4{acc[g][0], acc[g][1], acc[g][2], acc[g][3]};
      *reinterpret_cast<f32x4*>(&lds_acc[wave][g][8 * c + 4]) = f32x4{acc[g][4], acc[g][5], acc[g][6], acc[g][7]};
    }
    if (c == 0 || c == 4) { lds_ml[wave][c >> 2][0] = m; lds_ml[wave][c >> 2][1] = l; }
  }
  __syncthreads();
  {
    const int g = tid >> 8, d = tid & 255;
    float M = -INFINITY;
#pragma unroll
    for (int w = 0; w < AW; ++w) M = fmaxf(M, lds_ml[w][g][0]);
    float L = 0.f, o = 0.f;
#pragma unroll
    for (int w = 0; w < AW; ++w) {
      const float wt = __builtin_amdgcn_exp2f(lds_ml[w][g][0] - M);
      L = fmaf(wt, lds_ml[w][g][1], L);
      o = fmaf(wt, lds_acc[w][g][d], o);
    }
    const size_t row = ((size_t)b * HQ + kvh * 2 + g) * NSPLIT + split;
    reinterpret_cast<float*>(ws + WS_PO)[row * DH + d] = o;
    if (d == 0) { reinterpret_cast<float*>(ws + WS_PML)[row * 2] = M; reinterpret_cast<float*>(ws + WS_PML)[row * 2 + 1] = L; }
  }
  asm volatile("" :: "v"(warm));
}

__global__ void __launch_bounds__(256) k3_out(const u16* __restrict__ x, const u16* __restrict__ wo,
                                              const u16* __restrict__ wd, unsigned char* __restrict__ ws,
                                              u16* __restrict__ out) {
  __shared__ float act[2][DH];
  __shared__ float red[4][8][16];
  __shared__ u32 s_last;
  const int tid = threadIdx.x, lane = tid & 63, wave = tid >> 6;
  const int cg = blockIdx.x % K3_CG, kcx = blockIdx.x / K3_CG;
  const int cq = tid & 7, rg = tid >> 3;
  const u16* wbase = (kcx < HQ ? wo + (size_t)kcx * DH * DM : wd + (size_t)(kcx - HQ) * DH * DM) + cg * 64 + cq * 8;
  if (kcx < HQ) {
    const float* pml = reinterpret_cast<const float*>(ws + WS_PML);
    const float* po = reinterpret_cast<const float*>(ws + WS_PO);
    float a[2][NSPLIT];
#pragma unroll
    for (int b = 0; b < 2; ++b)
#pragma unroll
      for (int s = 0; s < NSPLIT; ++s) a[b][s] = po[(((size_t)b * HQ + kcx) * NSPLIT + s) * DH + tid];
    float o2[2];
#pragma unroll
    for (int b = 0; b < 2; ++b) {
      const size_t row0 = ((size_t)b * HQ + kcx) * NSPLIT;
      float M = -INFINITY;
#pragma unroll
      for (int s = 0; s < NSPLIT; ++s) M = fmaxf(M, pml[(row0 + s) * 2]);
      float L = 0.f, o = 0.f;
#pragma unroll
      for (int s = 0; s < NSPLIT; ++s) {
        const float wt = __builtin_amdgcn_exp2f(pml[(row0 + s) * 2] - M);
        L = fmaf(wt, pml[(row0 + s) * 2 + 1], L);
        o = fmaf(wt, a[b][s], o);
      }
      o2[b] = bfr(o / L);
    }
    act[0][tid] = o2[0]; act[1][tid] = o2[1];
  } else {
    const u16* h = reinterpret_cast<const u16*>(ws + WS_H);
#pragma unroll
    for (int b = 0; b < 2; ++b) act[b][tid] = bf2f(h[b * FF + (kcx - HQ) * DH + tid]);
  }
  u32x4 w[8];
#pragma unroll
  for (int i = 0; i < 8; ++i) w[i] = *reinterpret_cast<const u32x4*>(wbase + (size_t)(rg + 32 * i) * DM);
  __syncthreads();
  float acc[16];
#pragma unroll
  for (int j = 0; j < 16; ++j) acc[j] = 0.f;
#pragma unroll
  for (int i = 0; i < 8; ++i) {
    const float x0 = act[0][rg + 32 * i], x1 = act[1][rg + 32 * i];
#pragma unroll
    for (int j = 0; j < 4; ++j) {
      const float lo = bf_lo(w[i][j]), hi = bf_hi(w[i][j]);
      acc[2 * j] = fmaf(x0, lo, acc[2 * j]); acc[2 * j + 1] = fmaf(x0, hi, acc[2 * j + 1]);
      acc[8 + 2 * j] = fmaf(x1, lo, acc[8 + 2 * j]); acc[8 + 2 * j + 1] = fmaf(x1, hi, acc[8 + 2 * j + 1]);
    }
  }
#pragma unroll
  for (int j = 0; j < 16; ++j) {
    float t = acc[j];
    t += dpp<XOR8>(t);
    acc[j] = sum32x(sum16x(t));
  }
  if (lane < 8) {
#pragma unroll
    for (int j = 0; j < 16; j += 4) *reinterpret_cast<f32x4*>(&red[wave][lane][j]) = f32x4{acc[j], acc[j + 1], acc[j + 2], acc[j + 3]};
  }
  __syncthreads();
  float* slab = reinterpret_cast<float*>(ws + WS_SLAB) + (size_t)cg * K3_KC * 128;
  const int ob = tid >> 6, oc = tid & 63;
  if (tid < 128) {
    const int c8 = oc >> 3, j = oc & 7;
    const float t = (red[0][c8][ob * 8 + j] + red[1][c8][ob * 8 + j]) + (red[2][c8][ob * 8 + j] + red[3][c8][ob * 8 + j]);
    __hip_atomic_store(slab + kcx * 128 + tid, t, __ATOMIC_RELAXED, __HIP_MEMORY_SCOPE_AGENT);
  }
  asm volatile("s_waitcnt vmcnt(0)" ::: "memory");
  __syncthreads();
  if (tid == 0) {
    u32* cnt = reinterpret_cast<u32*>(ws + WS_CNT) + cg * 16;
    const u32 old = __hip_atomic_fetch_add(cnt, 1u, __ATOMIC_RELAXED, __HIP_MEMORY_SCOPE_AGENT);
    s_last = (old == (u32)(K3_KC - 1)) ? 1u : 0u;
  }
  __syncthreads();
  if (s_last == 0u) return;
  if (tid < 128) {
    float v[K3_KC];
#pragma unroll
    for (int k = 0; k < K3_KC; ++k) v[k] = __hip_atomic_load(slab + k * 128 + tid, __ATOMIC_RELAXED, __HIP_MEMORY_SCOPE_AGENT);
    float sa = 0.f, sf = 0.f;
#pragma unroll
    for (int k = 0; k < HQ; ++k) sa += v[k];
#pragma unroll
    for (int k = HQ; k < K3_KC; ++k) sf += v[k];
    const int n = cg * 64 + oc;
    const float xv = bf2f(x[ob * DM + n]);
    out[ob * DM + n] = f2bf(bfr(xv + bfr(sa)) + bfr(sf));
  }
}

extern "C" void kernel_launch(void* const* d_in, const int* in_sizes, int n_in,
                              void* d_out, int out_size, void* d_ws, size_t ws_size,
                              hipStream_t stream) {
  const u16* x = (const u16*)d_in[0];
  const u16* kc = (const u16*)d_in[1];
  const u16* vc = (const u16*)d_in[2];
  const u16* wq = (const u16*)d_in[3];
  const u16* wo = (const u16*)d_in[4];
  const u16* wg = (const u16*)d_in[6];
  const u16* wu = (const u16*)d_in[7];
  const u16* wd = (const u16*)d_in[8];
  unsigned char* ws = (unsigned char*)d_ws;
  k1_proj<<<K1_QWG + K1_FWG, 256, 0, stream>>>(x, wq, wg, wu, ws);
  k2_attn<<<NB * HKV * NSPLIT, AW * 64, 0, stream>>>(kc, vc, wo, wd, ws);
  k3_out<<<K3_CG * K3_KC, 256, 0, stream>>>(x, wo, wd, ws, (u16*)d_out);
}
```

```cpp
#include <hip/hip_runtime.h>
#include <hip/hip_bf16.h>
#include <stdint.h>
#define FUSEQ 0

typedef unsigned int u32;
typedef unsigned short u16;
using u32x4 = __attribute__((ext_vector_type(4))) unsigned;
using f32x4 = __attribute__((ext_vector_type(4))) float;
using bf16x2 = __attribute__((ext_vector_type(2))) __bf16;

constexpr int NB = 2, HQ = 16, HKV = 8, SK = 32768, DH = 256, DM = 448, FF = 1792;
constexpr int NQ = HQ * DH;
constexpr int NSPLIT = 16;
constexpr int AW = 8;
constexpr int STAGE = 8;
constexpr int NL = STAGE / 2;
constexpr int RKEYS = SK / NSPLIT;
constexpr int WSTG = RKEYS / (STAGE * AW);
constexpr float LOG2E = 1.4426950408889634f;

constexpr size_t WS_CNT = 0;
constexpr size_t WS_Q = 4096;
constexpr size_t WS_G = WS_Q + (size_t)NB * NQ * 2;
constexpr size_t WS_U = WS_G + (size_t)NB * FF * 2;
constexpr size_t WS_PML = 40960;
constexpr size_t WS_PO = 65536;
constexpr size_t WS_SLAB = WS_PO + (size_t)NB * HQ * NSPLIT * DH * 4;

__device__ __forceinline__ float bfr(float x) { return (float)(__bf16)x; }
__device__ __forceinline__ float bf_lo(u32 v) { return __uint_as_float(v << 16); }
__device__ __forceinline__ float bf_hi(u32 v) { return __uint_as_float(v & 0xffff0000u); }
__device__ __forceinline__ float bf2f(u16 v) { return __uint_as_float((u32)v << 16); }
__device__ __forceinline__ u16 f2bf(float x) { __bf16 b = (__bf16)x; return __builtin_bit_cast(u16, b); }
__device__ __forceinline__ float dot2(u32 k, u32 q, float acc) {
  return __builtin_amdgcn_fdot2_f32_bf16(__builtin_bit_cast(bf16x2, k), __builtin_bit_cast(bf16x2, q), acc, false);
}
template <int CTRL> __device__ __forceinline__ float dpp(float x) {
  return __builtin_bit_cast(float, __builtin_amdgcn_mov_dpp(__builtin_bit_cast(int, x), CTRL, 0xf, 0xf, true));
}
constexpr int XOR1 = 0xB1, XOR2 = 0x4E, XOR7 = 0x141, XOR8 = 0x128, ROR4 = 0x124;
__device__ __forceinline__ float sum16x(float x) {
  auto t = __builtin_amdgcn_permlane16_swap(__float_as_uint(x), __float_as_uint(x), false, false);
  return __uint_as_float(t[0]) + __uint_as_float(t[1]);
}
__device__ __forceinline__ float sum32x(float x) {
  auto t = __builtin_amdgcn_permlane32_swap(__float_as_uint(x), __float_as_uint(x), false, false);
  return __uint_as_float(t[0]) + __uint_as_float(t[1]);
}
__device__ __forceinline__ float wave_sum(float x) {
  x += dpp<XOR1>(x); x += dpp<XOR2>(x); x += dpp<XOR7>(x); x += dpp<XOR8>(x);
  return sum32x(sum16x(x));
}
__device__ __forceinline__ void ln_store(const float (&v)[7], float* xn, int lane) {
  float s = 0.f;
#pragma unroll
  for (int i = 0; i < 7; ++i) s += v[i];
  const float mean = wave_sum(s) * (1.0f / DM);
  float q = 0.f;
#pragma unroll
  for (int i = 0; i < 7; ++i) { const float t = v[i] - mean; q += t * t; }
  const float m_bf = bfr(mean);
  const float var_bf = bfr(wave_sum(q) * (1.0f / DM));
  const float den = bfr(sqrtf(bfr(var_bf + 1e-5f)));
#pragma unroll
  for (int i = 0; i < 7; ++i) xn[lane + 64 * i] = bfr(bfr(v[i] - m_bf) / den);
}

constexpr int K1_QWG = NQ / 32, K1_FWG = FF / 32;
__global__ void __launch_bounds__(256) k1_proj(const u16* __restrict__ x, const u16* __restrict__ wq,
                                               const u16* __restrict__ wg, const u16* __restrict__ wu,
                                               unsigned char* __restrict__ ws) {
  __shared__ float xn[NB * DM];
  __shared__ float red[4][4][16];
  const int tid = threadIdx.x, lane = tid & 63, wave = tid >> 6;
  const int wgid = blockIdx.x;
  const int cq = tid & 3, rg = tid >> 2;
  float v[7];
#pragma unroll
  for (int i = 0; i < 7; ++i) v[i] = bf2f(x[(wave & 1) * DM + lane + 64 * i]);
  const u16* wbase; u16* dst; int ld;
  if (wgid < K1_QWG) { wbase = wq + wgid * 32; dst = reinterpret_cast<u16*>(ws + WS_Q) + wgid * 32; ld = NQ; }
  else if (wgid < K1_QWG + K1_FWG) { const int f = (wgid - K1_QWG) * 32; wbase = wg + f; dst = reinterpret_cast<u16*>(ws + WS_G) + f; ld = FF; }
  else { const int f = (wgid - K1_QWG - K1_FWG) * 32; wbase = wu + f; dst = reinterpret_cast<u16*>(ws + WS_U) + f; ld = FF; }
  u32x4 w[7];
#pragma unroll
  for (int i = 0; i < 7; ++i) w[i] = *reinterpret_cast<const u32x4*>(wbase + cq * 8 + (size_t)(rg + 64 * i) * ld);
  if (wgid == 0 && tid < 8) reinterpret_cast<u32*>(ws + WS_CNT)[tid * 16] = 0u;
  if (wave < NB) ln_store(v, xn + wave * DM, lane);
  __syncthreads();
  float acc[16];
#pragma unroll
  for (int j = 0; j < 16; ++j) acc[j] = 0.f;
#pragma unroll
  for (int i = 0; i < 7; ++i) {
    const float x0 = xn[rg + 64 * i], x1 = xn[DM + rg + 64 * i];
#pragma unroll
    for (int j = 0; j < 4; ++j) {
      const float lo = bf_lo(w[i][j]), hi = bf_hi(w[i][j]);
      acc[2 * j] = fmaf(x0, lo, acc[2 * j]); acc[2 * j + 1] = fmaf(x0, hi, acc[2 * j + 1]);
      acc[8 + 2 * j] = fmaf(x1, lo, acc[8 + 2 * j]); acc[8 + 2 * j + 1] = fmaf(x1, hi, acc[8 + 2 * j + 1]);
    }
  }
#pragma unroll
  for (int j = 0; j < 16; ++j) {
    float t = acc[j];
    t += dpp<ROR4>(t); t += dpp<XOR8>(t);
    acc[j] = sum32x(sum16x(t));
  }
  if (lane < 4) {
#pragma unroll
    for (int j = 0; j < 16; j += 4) *reinterpret_cast<f32x4*>(&red[wave][lane][j]) = f32x4{acc[j], acc[j + 1], acc[j + 2], acc[j + 3]};
  }
  __syncthreads();
  if (tid < 64) {
    const int c4 = tid >> 4, idx = tid & 15;
    const float t = (red[0][c4][idx] + red[1][c4][idx]) + (red[2][c4][idx] + red[3][c4][idx]);
    dst[(idx >> 3) * ld + c4 * 8 + (idx & 7)] = f2bf(t);
  }
}

#define KV_LOAD(R, VOFF, SB) asm volatile("s_nop 4\n\t" \
  "global_load_dwordx4 %0, %4, %5 nt\n\t" \
  "global_load_dwordx4 %1, %4, %5 offset:1024 nt\n\t" \
  "global_load_dwordx4 %2, %4, %5 offset:2048 nt\n\t" \
  "global_load_dwordx4 %3, %4, %5 offset:3072 nt" \
  : "=&v"(R[0]), "=&v"(R[1]), "=&v"(R[2]), "=&v"(R[3]) : "v"(VOFF), "s"(SB) : "memory")
#define KV_WAIT(R, N) asm volatile("s_waitcnt vmcnt(%c4)" : "+v"(R[0]), "+v"(R[1]), "+v"(R[2]), "+v"(R[3]) : "i"(N) : "memory")

__device__ __forceinline__ float stg_qk(const u32x4 (&kk)[NL], const u32x4 (&q)[2], int lane) {
  const int c = lane & 31;
  float x[2 * NL];
#pragma unroll
  for (int i = 0; i < NL; ++i)
#pragma unroll
    for (int g = 0; g < 2; ++g) {
      float d = 0.f;
#pragma unroll
      for (int k = 0; k < 4; ++k) d = dot2(kk[i][k], q[g][k], d);
      x[i * 2 + g] = d;
    }
#pragma unroll
  for (int j = 0; j < NL; ++j) {
    auto t = __builtin_amdgcn_permlane16_swap(__float_as_uint(x[j]), __float_as_uint(x[j + NL]), false, false);
    x[j] = __uint_as_float(t[0]) + __uint_as_float(t[1]);
  }
#define TR_STEP(HALF, CTRL, BIT) _Pragma("unroll") for (int h = 0; h < (HALF); ++h) { \
    const float lo_ = x[h], hi_ = x[h + (HALF)]; const float t1_ = lo_ + dpp<CTRL>(lo_), t2_ = hi_ + dpp<CTRL>(hi_); \
    x[h] = (c & (BIT)) ? t2_ : t1_; }
  TR_STEP(2, XOR8, 8) TR_STEP(1, XOR7, 4)
#undef TR_STEP
  const float t = x[0] + dpp<XOR2>(x[0]);
  return t + dpp<XOR1>(t);
}
__device__ __forceinline__ void stg_softmax(float sc, float (&acc)[2][8], float& m, float& l, float* prow, int lane, f32x4 (&pr)[NL / 2]) {
  const int r = lane >> 5, c = lane & 31;
  sc = bfr(sc) * (0.0625f * LOG2E);
  float pm = fmaxf(sc, dpp<XOR8>(sc));
  {
    auto t = __builtin_amdgcn_permlane16_swap(__float_as_uint(pm), __float_as_uint(pm), false, false);
    pm = fmaxf(__uint_as_float(t[0]), __uint_as_float(t[1]));
    auto u = __builtin_amdgcn_permlane32_swap(__float_as_uint(pm), __float_as_uint(pm), false, false);
    pm = fmaxf(__uint_as_float(u[0]), __uint_as_float(u[1]));
  }
  const float mn = fmaxf(m, pm);
  const float alpha = __builtin_amdgcn_exp2f(m - mn);
  const float p = __builtin_amdgcn_exp2f(sc - mn);
  l = fmaf(l, alpha, p);
  m = mn;
  const float a0 = __builtin_bit_cast(float, __builtin_amdgcn_readlane(__builtin_bit_cast(int, alpha), 0));
  const float a1 = __builtin_bit_cast(float, __builtin_amdgcn_readlane(__builtin_bit_cast(int, alpha), 4));
  prow[r * STAGE + (c >> 2)] = p;
#pragma unroll
  for (int k = 0; k < 8; ++k) { acc[0][k] *= a0; acc[1][k] *= a1; }
#pragma unroll
  for (int u = 0; u < NL / 2; ++u) pr[u] = *reinterpret_cast<const f32x4*>(prow + r * STAGE + 4 * u);
}
__device__ __forceinline__ void stg_pv(const u32x4 (&vv)[NL], const f32x4 (&pr)[NL / 2], float (&acc)[2][8]) {
#pragma unroll
  for (int i = 0; i < NL; ++i) {
    const float p0 = pr[i >> 1][(i & 1) * 2], p1 = pr[i >> 1][(i & 1) * 2 + 1];
#pragma unroll
    for (int k = 0; k < 4; ++k) {
      const float lo = bf_lo(vv[i][k]), hi = bf_hi(vv[i][k]);
      acc[0][2 * k] = fmaf(p0, lo, acc[0][2 * k]); acc[0][2 * k + 1] = fmaf(p0, hi, acc[0][2 * k + 1]);
      acc[1][2 * k] = fmaf(p1, lo, acc[1][2 * k]); acc[1][2 * k + 1] = fmaf(p1, hi, acc[1][2 * k + 1]);
    }
  }
}

constexpr int K3_CG = 7, K3_KC = 23;
__global__ void __launch_bounds__(AW * 64, 2) k2_attn(const u16* __restrict__ x, const u16* __restrict__ wq,
                                                           const u16* __restrict__ wg, const u16* __restrict__ wu,
                                                           const u16* __restrict__ kc, const u16* __restrict__ vc,
                                                           unsigned char* __restrict__ ws) {
  __shared__ __attribute__((aligned(16))) float lds_p[AW][2 * STAGE];
  __shared__ __attribute__((aligned(16))) float lds_acc[AW][2][DH];
  __shared__ float lds_ml[AW][2][2];
  const int tid = threadIdx.x, lane = tid & 63, wave = __builtin_amdgcn_readfirstlane(tid >> 6);
  const int bid = blockIdx.x;
  const int split = bid & 15, bh = bid >> 4;
  const int b = bh >> 3, kvh = bh & 7;
  const int c = lane & 31;
  const char* kgb = reinterpret_cast<const char*>(kc + ((size_t)bh * SK + (size_t)split * RKEYS + (size_t)wave * STAGE) * DH);
  const char* vgb = reinterpret_cast<const char*>(vc + ((size_t)bh * SK + (size_t)split * RKEYS + (size_t)wave * STAGE) * DH);
  constexpr size_t SSTEP = (size_t)AW * STAGE * DH * 2;
  const u32 voff = lane * 16;
  u32x4 kk[NL], vv[NL];
  u32x4 q[2];
  {
    const u16* qp = reinterpret_cast<const u16*>(ws + WS_Q) + b * NQ + (kvh * 2) * DH + 8 * c;
    asm volatile("global_load_dwordx4 %0, %2, off\n\tglobal_load_dwordx4 %1, %2, off offset:512" : "=&v"(q[0]), "=&v"(q[1]) : "v"(qp) : "memory");
  }
  KV_LOAD(kk, voff, kgb); KV_LOAD(vv, voff, vgb);
  asm volatile("s_waitcnt vmcnt(%c2)" : "+v"(q[0]), "+v"(q[1]) : "i"(2 * NL) : "memory");
  float acc[2][8];
#pragma unroll
  for (int g = 0; g < 2; ++g)
#pragma unroll
    for (int k = 0; k < 8; ++k) acc[g][k] = 0.f;
  float m = -INFINITY, l = 0.f;
  float* prow = &lds_p[wave][0];
#pragma unroll 1
  for (int s = 0; s < WSTG; ++s) {
    const bool more = s + 1 < WSTG;
    const char* kb = more ? kgb + (size_t)(s + 1) * SSTEP : reinterpret_cast<const char*>(kc);
    const char* vb = more ? vgb + (size_t)(s + 1) * SSTEP : reinterpret_cast<const char*>(kc);
    KV_WAIT(kk, NL);
    const float sc = stg_qk(kk, q, lane);
    KV_LOAD(kk, voff, kb);
    f32x4 pr[NL / 2];
    stg_softmax(sc, acc, m, l, prow, lane, pr);
    KV_WAIT(vv, NL);
    stg_pv(vv, pr, acc);
    KV_LOAD(vv, voff, vb);
  }
  KV_WAIT(kk, 0); KV_WAIT(vv, 0);
#pragma unroll
  for (int g = 0; g < 2; ++g)
#pragma unroll
    for (int k = 0; k < 8; ++k) acc[g][k] = sum32x(acc[g][k]);
  l = sum32x(sum16x(l + dpp<XOR8>(l)));
  if (lane < 32) {
#pragma unroll
    for (int g = 0; g < 2; ++g) {
      *reinterpret_cast<f32x4*>(&lds_acc[wave][g][8 * c]) = f32x4{acc[g][0], acc[g][1], acc[g][2], acc[g][3]};
      *reinterpret_cast<f32x4*>(&lds_acc[wave][g][8 * c + 4]) = f32x4{acc[g][4], acc[g][5], acc[g][6], acc[g][7]};
    }
    if (c == 0 || c == 4) { lds_ml[wave][c >> 2][0] = m; lds_ml[wave][c >> 2][1] = l; }
  }
  __syncthreads();
  {
    const int g = tid >> 8, d = tid & 255;
    float M = -INFINITY;
#pragma unroll
    for (int w = 0; w < AW; ++w) M = fmaxf(M, lds_ml[w][g][0]);
    float L = 0.f, o = 0.f;
#pragma unroll
    for (int w = 0; w < AW; ++w) {
      const float wt = __builtin_amdgcn_exp2f(lds_ml[w][g][0] - M);
      L = fmaf(wt, lds_ml[w][g][1], L);
      o = fmaf(wt, lds_acc[w][g][d], o);
    }
    const size_t row = ((size_t)b * HQ + kvh * 2 + g) * NSPLIT + split;
    reinterpret_cast<float*>(ws + WS_PO)[row * DH + d] = o;
    if (d == 0) { reinterpret_cast<float*>(ws + WS_PML)[row * 2] = M; reinterpret_cast<float*>(ws + WS_PML)[row * 2 + 1] = L; }
  }
}

__global__ void __launch_bounds__(256) k3_out(const u16* __restrict__ x, const u16* __restrict__ wo,
                                              const u16* __restrict__ wd, unsigned char* __restrict__ ws,
                                              u16* __restrict__ out) {
  __shared__ float act[2][DH];
  __shared__ float red[4][8][16];
  __shared__ u32 s_last;
  const int tid = threadIdx.x, lane = tid & 63, wave = tid >> 6;
  const int cg = blockIdx.x % K3_CG, kcx = blockIdx.x / K3_CG;
  const int cq = tid & 7, rg = tid >> 3;
  const u16* wbase = (kcx < HQ ? wo + (size_t)kcx * DH * DM : wd + (size_t)(kcx - HQ) * DH * DM) + cg * 64 + cq * 8;
  u32x4 w[8];
#pragma unroll
  for (int i = 0; i < 8; ++i) w[i] = *reinterpret_cast<const u32x4*>(wbase + (size_t)(rg + 32 * i) * DM);
  if (kcx < HQ) {
    const float* pml = reinterpret_cast<const float*>(ws + WS_PML);
    const float* po = reinterpret_cast<const float*>(ws + WS_PO);
    float a[2][NSPLIT];
#pragma unroll
    for (int b = 0; b < 2; ++b)
#pragma unroll
      for (int s = 0; s < NSPLIT; ++s) a[b][s] = po[(((size_t)b * HQ + kcx) * NSPLIT + s) * DH + tid];
    float o2[2];
#pragma unroll
    for (int b = 0; b < 2; ++b) {
      const size_t row0 = ((size_t)b * HQ + kcx) * NSPLIT;
      float M = -INFINITY;
#pragma unroll
      for (int s = 0; s < NSPLIT; ++s) M = fmaxf(M, pml[(row0 + s) * 2]);
      float L = 0.f, o = 0.f;
#pragma unroll
      for (int s = 0; s < NSPLIT; ++s) {
        const float wt = __builtin_amdgcn_exp2f(pml[(row0 + s) * 2] - M);
        L = fmaf(wt, pml[(row0 + s) * 2 + 1], L);
        o = fmaf(wt, a[b][s], o);
      }
      o2[b] = bfr(o / L);
    }
    act[0][tid] = o2[0]; act[1][tid] = o2[1];
  } else {
    const u16* gp = reinterpret_cast<const u16*>(ws + WS_G) + (kcx - HQ) * DH + tid;
    const u16* up = reinterpret_cast<const u16*>(ws + WS_U) + (kcx - HQ) * DH + tid;
#pragma unroll
    for (int b = 0; b < 2; ++b) {
      const float g = bf2f(gp[b * FF]), u = bf2f(up[b * FF]);
      const float inner = 0.7978845608028654f * (g + 0.044715f * g * g * g);
      const float ge = bfr(0.5f * g * (1.0f + tanhf(inner)));
      act[b][tid] = bfr(ge * u);
    }
  }
  __syncthreads();
  float acc[16];
#pragma unroll
  for (int j = 0; j < 16; ++j) acc[j] = 0.f;
#pragma unroll
  for (int i = 0; i < 8; ++i) {
    const float x0 = act[0][rg + 32 * i], x1 = act[1][rg + 32 * i];
#pragma unroll
    for (int j = 0; j < 4; ++j) {
      const float lo = bf_lo(w[i][j]), hi = bf_hi(w[i][j]);
      acc[2 * j] = fmaf(x0, lo, acc[2 * j]); acc[2 * j + 1] = fmaf(x0, hi, acc[2 * j + 1]);
      acc[8 + 2 * j] = fmaf(x1, lo, acc[8 + 2 * j]); acc[8 + 2 * j + 1] = fmaf(x1, hi, acc[8 + 2 * j + 1]);
    }
  }
#pragma unroll
  for (int j = 0; j < 16; ++j) {
    float t = acc[j];
    t += dpp<XOR8>(t);
    acc[j] = sum32x(sum16x(t));
  }
  if (lane < 8) {
#pragma unroll
    for (int j = 0; j < 16; j += 4) *reinterpret_cast<f32x4*>(&red[wave][lane][j]) = f32x4{acc[j], acc[j + 1], acc[j + 2], acc[j + 3]};
  }
  __syncthreads();
  float* slab = reinterpret_cast<float*>(ws + WS_SLAB) + (size_t)cg * K3_KC * 128;
  const int ob = tid >> 6, oc = tid & 63;
  if (tid < 128) {
    const int c8 = oc >> 3, j = oc & 7;
    const float t = (red[0][c8][ob * 8 + j] + red[1][c8][ob * 8 + j]) + (red[2][c8][ob * 8 + j] + red[3][c8][ob * 8 + j]);
    __hip_atomic_store(slab + kcx * 128 + tid, t, __ATOMIC_RELAXED, __HIP_MEMORY_SCOPE_AGENT);
  }
  asm volatile("s_waitcnt vmcnt(0)" ::: "memory");
  __syncthreads();
  if (tid == 0) {
    u32* cnt = reinterpret_cast<u32*>(ws + WS_CNT) + cg * 16;
    const u32 old = __hip_atomic_fetch_add(cnt, 1u, __ATOMIC_RELAXED, __HIP_MEMORY_SCOPE_AGENT);
    s_last = (old == (u32)(K3_KC - 1)) ? 1u : 0u;
  }
  __syncthreads();
  if (s_last == 0u) return;
  if (tid < 128) {
    float v[K3_KC];
#pragma unroll
    for (int k = 0; k < K3_KC; ++k) v[k] = __hip_atomic_load(slab + k * 128 + tid, __ATOMIC_RELAXED, __HIP_MEMORY_SCOPE_AGENT);
    float sa = 0.f, sf = 0.f;
#pragma unroll
    for (int k = 0; k < HQ; ++k) sa += v[k];
#pragma unroll
    for (int k = HQ; k < K3_KC; ++k) sf += v[k];
    const int n = cg * 64 + oc;
    const float xv = bf2f(x[ob * DM + n]);
    out[ob * DM + n] = f2bf(bfr(xv + bfr(sa)) + bfr(sf));
  }
}

extern "C" void kernel_launch(void* const* d_in, const int* in_sizes, int n_in,
                              void* d_out, int out_size, void* d_ws, size_t ws_size,
                              hipStream_t stream) {
  const u16* x = (const u16*)d_in[0];
  const u16* kc = (const u16*)d_in[1];
  const u16* vc = (const u16*)d_in[2];
  const u16* wq = (const u16*)d_in[3];
  const u16* wo = (const u16*)d_in[4];
  const u16* wg = (const u16*)d_in[6];
  const u16* wu = (const u16*)d_in[7];
  const u16* wd = (const u16*)d_in[8];
  unsigned char* ws = (unsigned char*)d_ws;
  k1_proj<<<K1_QWG + 2 * K1_FWG, 256, 0, stream>>>(x, wq, wg, wu, ws);
  k2_attn<<<NB * HKV * NSPLIT, AW * 64, 0, stream>>>(x, wq, wg, wu, kc, vc, ws);
  k3_out<<<K3_CG * K3_KC, 256, 0, stream>>>(x, wo, wd, ws, (u16*)d_out);
}
```

```cpp
#include <hip/hip_runtime.h>
#include <hip/hip_bf16.h>
#include <stdint.h>

typedef unsigned int u32;
typedef unsigned short u16;
using u32x4 = __attribute__((ext_vector_type(4))) unsigned;
using f32x4 = __attribute__((ext_vector_type(4))) float;
using bf16x2 = __attribute__((ext_vector_type(2))) __bf16;

constexpr int NB = 2, HQ = 16, HKV = 8, SK = 32768, DH = 256, DM = 448, FF = 1792;
constexpr int NQ = HQ * DH;
constexpr int NSPLIT = 16;
constexpr int AW = 4;
constexpr int STAGE = 8;
constexpr int NL = STAGE / 2;
constexpr int RKEYS = SK / NSPLIT;
constexpr int WSTG = RKEYS / (STAGE * AW);
constexpr float LOG2E = 1.4426950408889634f;

constexpr size_t WS_CNT = 0;
constexpr size_t WS_Q = 4096;
constexpr size_t WS_G = WS_Q + (size_t)NB * NQ * 2;
constexpr size_t WS_U = WS_G + (size_t)NB * FF * 2;
constexpr size_t WS_PML = 40960;
constexpr size_t WS_PO = 65536;
constexpr size_t WS_SLAB = WS_PO + (size_t)NB * HQ * NSPLIT * DH * 4;
static_assert(WS_U + (size_t)NB * FF * 2 <= WS_PML && WS_PML + (size_t)NB * HQ * NSPLIT * 8 <= WS_PO, "workspace map");

__device__ __forceinline__ float bfr(float x) { return (float)(__bf16)x; }
__device__ __forceinline__ float bf_lo(u32 v) { return __uint_as_float(v << 16); }
__device__ __forceinline__ float bf_hi(u32 v) { return __uint_as_float(v & 0xffff0000u); }
__device__ __forceinline__ float bf2f(u16 v) { return __uint_as_float((u32)v << 16); }
__device__ __forceinline__ u16 f2bf(float x) { __bf16 b = (__bf16)x; return __builtin_bit_cast(u16, b); }
__device__ __forceinline__ float dot2(u32 k, u32 q, float acc) {
  return __builtin_amdgcn_fdot2_f32_bf16(__builtin_bit_cast(bf16x2, k), __builtin_bit_cast(bf16x2, q), acc, false);
}
template <int CTRL> __device__ __forceinline__ float dpp(float x) {
  return __builtin_bit_cast(float, __builtin_amdgcn_mov_dpp(__builtin_bit_cast(int, x), CTRL, 0xf, 0xf, true));
}
constexpr int XOR1 = 0xB1, XOR2 = 0x4E, XOR7 = 0x141, XOR8 = 0x128, ROR4 = 0x124;
__device__ __forceinline__ float sum16x(float x) {
  auto t = __builtin_amdgcn_permlane16_swap(__float_as_uint(x), __float_as_uint(x), false, false);
  return __uint_as_float(t[0]) + __uint_as_float(t[1]);
}
__device__ __forceinline__ float sum32x(float x) {
  auto t = __builtin_amdgcn_permlane32_swap(__float_as_uint(x), __float_as_uint(x), false, false);
  return __uint_as_float(t[0]) + __uint_as_float(t[1]);
}
__device__ __forceinline__ float wave_sum(float x) {
  x += dpp<XOR1>(x); x += dpp<XOR2>(x); x += dpp<XOR7>(x); x += dpp<XOR8>(x);
  return sum32x(sum16x(x));
}
__device__ __forceinline__ void ln_store(const float (&v)[7], float* xn, int lane) {
  float s = 0.f;
#pragma unroll
  for (int i = 0; i < 7; ++i) s += v[i];
  const float mean = wave_sum(s) * (1.0f / DM);
  float q = 0.f;
#pragma unroll
  for (int i = 0; i < 7; ++i) { const float t = v[i] - mean; q += t * t; }
  const float m_bf = bfr(mean);
  const float var_bf = bfr(wave_sum(q) * (1.0f / DM));
  const float den = bfr(sqrtf(bfr(var_bf + 1e-5f)));
#pragma unroll
  for (int i = 0; i < 7; ++i) xn[lane + 64 * i] = bfr(bfr(v[i] - m_bf) / den);
}

constexpr int K1_QWG = NQ / 32, K1_FWG = FF / 32;
__global__ void __launch_bounds__(256) k1_proj(const u16* __restrict__ x, const u16* __restrict__ wq,
                                               const u16* __restrict__ wg, const u16* __restrict__ wu,
                                               unsigned char* __restrict__ ws) {
  __shared__ float xn[NB * DM];
  __shared__ float red[4][4][16];
  const int tid = threadIdx.x, lane = tid & 63, wave = tid >> 6;
  const int wgid = blockIdx.x;
  const int cq = tid & 3, rg = tid >> 2;
  float v[7];
#pragma unroll
  for (int i = 0; i < 7; ++i) v[i] = bf2f(x[(wave & 1) * DM + lane + 64 * i]);
  const u16* wbase; u16* dst; int ld;
  if (wgid < K1_QWG) { wbase = wq + wgid * 32; dst = reinterpret_cast<u16*>(ws + WS_Q) + wgid * 32; ld = NQ; }
  else if (wgid < K1_QWG + K1_FWG) { const int f = (wgid - K1_QWG) * 32; wbase = wg + f; dst = reinterpret_cast<u16*>(ws + WS_G) + f; ld = FF; }
  else { const int f = (wgid - K1_QWG - K1_FWG) * 32; wbase = wu + f; dst = reinterpret_cast<u16*>(ws + WS_U) + f; ld = FF; }
  u32x4 w[7];
#pragma unroll
  for (int i = 0; i < 7; ++i) w[i] = *reinterpret_cast<const u32x4*>(wbase + cq * 8 + (size_t)(rg + 64 * i) * ld);
  if (wgid == 0 && tid < 8) reinterpret_cast<u32*>(ws + WS_CNT)[tid * 16] = 0u;
  if (wave < NB) ln_store(v, xn + wave * DM, lane);
  __syncthreads();
  float acc[16];
#pragma unroll
  for (int j = 0; j < 16; ++j) acc[j] = 0.f;
#pragma unroll
  for (int i = 0; i < 7; ++i) {
    const float x0 = xn[rg + 64 * i], x1 = xn[DM + rg + 64 * i];
#pragma unroll
    for (int j = 0; j < 4; ++j) {
      const float lo = bf_lo(w[i][j]), hi = bf_hi(w[i][j]);
      acc[2 * j] = fmaf(x0, lo, acc[2 * j]); acc[2 * j + 1] = fmaf(x0, hi, acc[2 * j + 1]);
      acc[8 + 2 * j] = fmaf(x1, lo, acc[8 + 2 * j]); acc[8 + 2 * j + 1] = fmaf(x1, hi, acc[8 + 2 * j + 1]);
    }
  }
#pragma unroll
  for (int j = 0; j < 16; ++j) {
    float t = acc[j];
    t += dpp<ROR4>(t); t += dpp<XOR8>(t);
    acc[j] = sum32x(sum16x(t));
  }
  if (lane < 4) {
#pragma unroll
    for (int j = 0; j < 16; j += 4) *reinterpret_cast<f32x4*>(&red[wave][lane][j]) = f32x4{acc[j], acc[j + 1], acc[j + 2], acc[j + 3]};
  }
  __syncthreads();
  if (tid < 64) {
    const int c4 = tid >> 4, idx = tid & 15;
    const float t = (red[0][c4][idx] + red[1][c4][idx]) + (red[2][c4][idx] + red[3][c4][idx]);
    dst[(idx >> 3) * ld + c4 * 8 + (idx & 7)] = f2bf(t);
  }
}

#define KV_LOAD(R, VOFF, SB) asm volatile("s_nop 4\n\t" \
  "global_load_dwordx4 %0, %4, %5 nt\n\t" \
  "global_load_dwordx4 %1, %4, %5 offset:1024 nt\n\t" \
  "global_load_dwordx4 %2, %4, %5 offset:2048 nt\n\t" \
  "global_load_dwordx4 %3, %4, %5 offset:3072 nt" \
  : "=&v"(R[0]), "=&v"(R[1]), "=&v"(R[2]), "=&v"(R[3]) : "v"(VOFF), "s"(SB) : "memory")
#define KV_WAIT(R, N) asm volatile("s_waitcnt vmcnt(%c4)" : "+v"(R[0]), "+v"(R[1]), "+v"(R[2]), "+v"(R[3]) : "i"(N) : "memory")

__device__ __forceinline__ float stg_qk(const u32x4 (&kk)[NL], const u32x4 (&q)[2], int lane) {
  const int c = lane & 31;
  float x[2 * NL];
#pragma unroll
  for (int i = 0; i < NL; ++i)
#pragma unroll
    for (int g = 0; g < 2; ++g) {
      float d = 0.f;
#pragma unroll
      for (int k = 0; k < 4; ++k) d = dot2(kk[i][k], q[g][k], d);
      x[i * 2 + g] = d;
    }
#pragma unroll
  for (int j = 0; j < NL; ++j) {
    auto t = __builtin_amdgcn_permlane16_swap(__float_as_uint(x[j]), __float_as_uint(x[j + NL]), false, false);
    x[j] = __uint_as_float(t[0]) + __uint_as_float(t[1]);
  }
#define TR_STEP(HALF, CTRL, BIT) _Pragma("unroll") for (int h = 0; h < (HALF); ++h) { \
    const float lo_ = x[h], hi_ = x[h + (HALF)]; const float t1_ = lo_ + dpp<CTRL>(lo_), t2_ = hi_ + dpp<CTRL>(hi_); \
    x[h] = (c & (BIT)) ? t2_ : t1_; }
  TR_STEP(2, XOR8, 8) TR_STEP(1, XOR7, 4)
#undef TR_STEP
  const float t = x[0] + dpp<XOR2>(x[0]);
  return t + dpp<XOR1>(t);
}
__device__ __forceinline__ void stg_softmax(float sc, float (&acc)[2][8], float& m, float& l, float* prow, int lane, f32x4 (&pr)[NL / 2]) {
  const int r = lane >> 5, c = lane & 31;
  sc = bfr(sc) * (0.0625f * LOG2E);
  float pm = fmaxf(sc, dpp<XOR8>(sc));
  {
    auto t = __builtin_amdgcn_permlane16_swap(__float_as_uint(pm), __float_as_uint(pm), false, false);
    pm = fmaxf(__uint_as_float(t[0]), __uint_as_float(t[1]));
    auto u = __builtin_amdgcn_permlane32_swap(__float_as_uint(pm), __float_as_uint(pm), false, false);
    pm = fmaxf(__uint_as_float(u[0]), __uint_as_float(u[1]));
  }
  const float mn = fmaxf(m, pm);
  const float alpha = __builtin_amdgcn_exp2f(m - mn);
  const float p = __builtin_amdgcn_exp2f(sc - mn);
  l = fmaf(l, alpha, p);
  m = mn;
  const float a0 = __builtin_bit_cast(float, __builtin_amdgcn_readlane(__builtin_bit_cast(int, alpha), 0));
  const float a1 = __builtin_bit_cast(float, __builtin_amdgcn_readlane(__builtin_bit_cast(int, alpha), 4));
  prow[r * STAGE + (c >> 2)] = p;
#pragma unroll
  for (int k = 0; k < 8; ++k) { acc[0][k] *= a0; acc[1][k] *= a1; }
#pragma unroll
  for (int u = 0; u < NL / 2; ++u) pr[u] = *reinterpret_cast<const f32x4*>(prow + r * STAGE + 4 * u);
}
__device__ __forceinline__ void stg_pv(const u32x4 (&vv)[NL], const f32x4 (&pr)[NL / 2], float (&acc)[2][8]) {
#pragma unroll
  for (int i = 0; i < NL; ++i) {
    const float p0 = pr[i >> 1][(i & 1) * 2], p1 = pr[i >> 1][(i & 1) * 2 + 1];
#pragma unroll
    for (int k = 0; k < 4; ++k) {
      const float lo = bf_lo(vv[i][k]), hi = bf_hi(vv[i][k]);
      acc[0][2 * k] = fmaf(p0, lo, acc[0][2 * k]); acc[0][2 * k + 1] = fmaf(p0, hi, acc[0][2 * k + 1]);
      acc[1][2 * k] = fmaf(p1, lo, acc[1][2 * k]); acc[1][2 * k + 1] = fmaf(p1, hi, acc[1][2 * k + 1]);
    }
  }
}

__global__ void __launch_bounds__(AW * 64) k2_attn(const u16* __restrict__ kc, const u16* __restrict__ vc,
                                                   unsigned char* __restrict__ ws) {
  __shared__ __attribute__((aligned(16))) float lds_p[AW][2 * STAGE];
  __shared__ __attribute__((aligned(16))) float lds_acc[AW][2][DH];
  __shared__ float lds_ml[AW][2][2];
  const int tid = threadIdx.x, lane = tid & 63, wave = __builtin_amdgcn_readfirstlane(tid >> 6);
  const int bid = blockIdx.x;
  const int split = bid & 15, bh = bid >> 4;
  const int b = bh >> 3, kvh = bh & 7;
  const int c = lane & 31;
  const char* kgb = reinterpret_cast<const char*>(kc + ((size_t)bh * SK + (size_t)split * RKEYS + (size_t)wave * STAGE) * DH);
  const char* vgb = reinterpret_cast<const char*>(vc + ((size_t)bh * SK + (size_t)split * RKEYS + (size_t)wave * STAGE) * DH);
  constexpr size_t SSTEP = (size_t)AW * STAGE * DH * 2;
  const u32 voff = lane * 16;
  u32x4 kk[NL], vv[NL];
  u32x4 q[2];
  {
    const u16* qp = reinterpret_cast<const u16*>(ws + WS_Q) + b * NQ + (kvh * 2) * DH + 8 * c;
    asm volatile("global_load_dwordx4 %0, %2, off\n\tglobal_load_dwordx4 %1, %2, off offset:512" : "=&v"(q[0]), "=&v"(q[1]) : "v"(qp) : "memory");
  }
  KV_LOAD(kk, voff, kgb); KV_LOAD(vv, voff, vgb);
  asm volatile("s_waitcnt vmcnt(%c2)" : "+v"(q[0]), "+v"(q[1]) : "i"(2 * NL) : "memory");
  float acc[2][8];
#pragma unroll
  for (int g = 0; g < 2; ++g)
#pragma unroll
    for (int k = 0; k < 8; ++k) acc[g][k] = 0.f;
  float m = -INFINITY, l = 0.f;
  float* prow = &lds_p[wave][0];
#pragma unroll 1
  for (int s = 0; s < WSTG; ++s) {
    const bool more = s + 1 < WSTG;
    const char* kb = more ? kgb + (size_t)(s + 1) * SSTEP : reinterpret_cast<const char*>(kc);
    const char* vb = more ? vgb + (size_t)(s + 1) * SSTEP : reinterpret_cast<const char*>(kc);
    KV_WAIT(kk, NL);
    const float sc = stg_qk(kk, q, lane);
    KV_LOAD(kk, voff, kb);
    f32x4 pr[NL / 2];
    stg_softmax(sc, acc, m, l, prow, lane, pr);
    KV_WAIT(vv, NL);
    stg_pv(vv, pr, acc);
    KV_LOAD(vv, voff, vb);
  }
  KV_WAIT(kk, 0); KV_WAIT(vv, 0);
#pragma unroll
  for (int g = 0; g < 2; ++g)
#pragma unroll
    for (int k = 0; k < 8; ++k) acc[g][k] = sum32x(acc[g][k]);
  l = sum32x(sum16x(l + dpp<XOR8>(l)));
  if (lane < 32) {
#pragma unroll
    for (int g = 0; g < 2; ++g) {
      *reinterpret_cast<f32x4*>(&lds_acc[wave][g][8 * c]) = f32x4{acc[g][0], acc[g][1], acc[g][2], acc[g][3]};
      *reinterpret_cast<f32x4*>(&lds_acc[wave][g][8 * c + 4]) = f32x4{acc[g][4], acc[g][5], acc[g][6], acc[g][7]};
    }
    if (c == 0 || c == 4) { lds_ml[wave][c >> 2][0] = m; lds_ml[wave][c >> 2][1] = l; }
  }
  __syncthreads();
#pragma unroll
  for (int g = 0; g < 2; ++g) {
    float M = -INFINITY;
#pragma unroll
    for (int w = 0; w < AW; ++w) M = fmaxf(M, lds_ml[w][g][0]);
    float L = 0.f, o = 0.f;
#pragma unroll
    for (int w = 0; w < AW; ++w) {
      const float wt = __builtin_amdgcn_exp2f(lds_ml[w][g][0] - M);
      L = fmaf(wt, lds_ml[w][g][1], L);
      o = fmaf(wt, lds_acc[w][g][tid], o);
    }
    const size_t row = ((size_t)b * HQ + kvh * 2 + g) * NSPLIT + split;
    reinterpret_cast<float*>(ws + WS_PO)[row * DH + tid] = o;
    if (tid == 0) { reinterpret_cast<float*>(ws + WS_PML)[row * 2] = M; reinterpret_cast<float*>(ws + WS_PML)[row * 2 + 1] = L; }
  }
}

constexpr int K3_CG = 7, K3_KC = 23;
__global__ void __launch_bounds__(256) k3_out(const u16* __restrict__ x, const u16* __restrict__ wo,
                                              const u16* __restrict__ wd, unsigned char* __restrict__ ws,
                                              u16* __restrict__ out) {
  __shared__ float act[2][DH];
  __shared__ float red[4][8][16];
  __shared__ u32 s_last;
  const int tid = threadIdx.x, lane = tid & 63, wave = tid >> 6;
  const int cg = blockIdx.x % K3_CG, kcx = blockIdx.x / K3_CG;
  const int cq = tid & 7, rg = tid >> 3;
  const u16* wbase = (kcx < HQ ? wo + (size_t)kcx * DH * DM : wd + (size_t)(kcx - HQ) * DH * DM) + cg * 64 + cq * 8;
  u32x4 w[8];
#pragma unroll
  for (int i = 0; i < 8; ++i) w[i] = *reinterpret_cast<const u32x4*>(wbase + (size_t)(rg + 32 * i) * DM);
  if (kcx < HQ) {
    const float* pml = reinterpret_cast<const float*>(ws + WS_PML);
    const float* po = reinterpret_cast<const float*>(ws + WS_PO);
    float a[2][NSPLIT];
#pragma unroll
    for (int b = 0; b < 2; ++b)
#pragma unroll
      for (int s = 0; s < NSPLIT; ++s) a[b][s] = po[(((size_t)b * HQ + kcx) * NSPLIT + s) * DH + tid];
    float o2[2];
#pragma unroll
    for (int b = 0; b < 2; ++b) {
      const size_t row0 = ((size_t)b * HQ + kcx) * NSPLIT;
      float M = -INFINITY;
#pragma unroll
      for (int s = 0; s < NSPLIT; ++s) M = fmaxf(M, pml[(row0 + s) * 2]);
      float L = 0.f, o = 0.f;
#pragma unroll
      for (int s = 0; s < NSPLIT; ++s) {
        const float wt = __builtin_amdgcn_exp2f(pml[(row0 + s) * 2] - M);
        L = fmaf(wt, pml[(row0 + s) * 2 + 1], L);
        o = fmaf(wt, a[b][s], o);
      }
      o2[b] = bfr(o / L);
    }
    act[0][tid] = o2[0]; act[1][tid] = o2[1];
  } else {
    const u16* gp = reinterpret_cast<const u16*>(ws + WS_G) + (kcx - HQ) * DH + tid;
    const u16* up = reinterpret_cast<const u16*>(ws + WS_U) + (kcx - HQ) * DH + tid;
#pragma unroll
    for (int b = 0; b < 2; ++b) {
      const float g = bf2f(gp[b * FF]), u = bf2f(up[b * FF]);
      const float inner = 0.7978845608028654f * (g + 0.044715f * g * g * g);
      const float ge = bfr(0.5f * g * (1.0f + tanhf(inner)));
      act[b][tid] = bfr(ge * u);
    }
  }
  __syncthreads();
  float acc[16];
#pragma unroll
  for (int j = 0; j < 16; ++j) acc[j] = 0.f;
#pragma unroll
  for (int i = 0; i < 8; ++i) {
    const float x0 = act[0][rg + 32 * i], x1 = act[1][rg + 32 * i];
#pragma unroll
    for (int j = 0; j < 4; ++j) {
      const float lo = bf_lo(w[i][j]), hi = bf_hi(w[i][j]);
      acc[2 * j] = fmaf(x0, lo, acc[2 * j]); acc[2 * j + 1] = fmaf(x0, hi, acc[2 * j + 1]);
      acc[8 + 2 * j] = fmaf(x1, lo, acc[8 + 2 * j]); acc[8 + 2 * j + 1] = fmaf(x1, hi, acc[8 + 2 * j + 1]);
    }
  }
#pragma unroll
  for (int j = 0; j < 16; ++j) {
    float t = acc[j];
    t += dpp<XOR8>(t);
    acc[j] = sum32x(sum16x(t));
  }
  if (lane < 8) {
#pragma unroll
    for (int j = 0; j < 16; j += 4) *reinterpret_cast<f32x4*>(&red[wave][lane][j]) = f32x4{acc[j], acc[j + 1], acc[j + 2], acc[j + 3]};
  }
  __syncthreads();
  float* slab = reinterpret_cast<float*>(ws + WS_SLAB) + (size_t)cg * K3_KC * 128;
  const int ob = tid >> 6, oc = tid & 63;
  if (tid < 128) {
    const int c8 = oc >> 3, j = oc & 7;
    const float t = (red[0][c8][ob * 8 + j] + red[1][c8][ob * 8 + j]) + (red[2][c8][ob * 8 + j] + red[3][c8][ob * 8 + j]);
    __hip_atomic_store(slab + kcx * 128 + tid, t, __ATOMIC_RELAXED, __HIP_MEMORY_SCOPE_AGENT);
  }
  asm volatile("s_waitcnt vmcnt(0)" ::: "memory");
  __syncthreads();
  if (tid == 0) {
    u32* cnt = reinterpret_cast<u32*>(ws + WS_CNT) + cg * 16;
    const u32 old = __hip_atomic_fetch_add(cnt, 1u, __ATOMIC_RELAXED, __HIP_MEMORY_SCOPE_AGENT);
    s_last = (old == (u32)(K3_KC - 1)) ? 1u : 0u;
  }
  __syncthreads();
  if (s_last == 0u) return;
  if (tid < 128) {
    float v[K3_KC];
#pragma unroll
    for (int k = 0; k < K3_KC; ++k) v[k] = __hip_atomic_load(slab + k * 128 + tid, __ATOMIC_RELAXED, __HIP_MEMORY_SCOPE_AGENT);
    float sa = 0.f, sf = 0.f;
#pragma unroll
    for (int k = 0; k < HQ; ++k) sa += v[k];
#pragma unroll
    for (int k = HQ; k < K3_KC; ++k) sf += v[k];
    const int n = cg * 64 + oc;
    const float xv = bf2f(x[ob * DM + n]);
    out[ob * DM + n] = f2bf(bfr(xv + bfr(sa)) + bfr(sf));
  }
}

extern "C" void kernel_launch(void* const* d_in, const int* in_sizes, int n_in,
                              void* d_out, int out_size, void* d_ws, size_t ws_size,
                              hipStream_t stream) {
  const u16* x = (const u16*)d_in[0];
  const u16* kc = (const u16*)d_in[1];
  const u16* vc = (const u16*)d_in[2];
  const u16* wq = (const u16*)d_in[3];
  const u16* wo = (const u16*)d_in[4];
  const u16* wg = (const u16*)d_in[6];
  const u16* wu = (const u16*)d_in[7];
  const u16* wd = (const u16*)d_in[8];
  unsigned char* ws = (unsigned char*)d_ws;
  k1_proj<<<K1_QWG + 2 * K1_FWG, 256, 0, stream>>>(x, wq, wg, wu, ws);
  k2_attn<<<NB * HKV * NSPLIT, AW * 64, 0, stream>>>(kc, vc, ws);
  k3_out<<<K3_CG * K3_KC, 256, 0, stream>>>(x, wo, wd, ws, (u16*)d_out);
}
```

```cpp
#include <hip/hip_runtime.h>
#include <hip/hip_bf16.h>
#include <stdint.h>

typedef unsigned int u32;
typedef unsigned short u16;
using u32x4 = __attribute__((ext_vector_type(4))) unsigned;
using f32x4 = __attribute__((ext_vector_type(4))) float;
using bf16x2 = __attribute__((ext_vector_type(2))) __bf16;

constexpr int NB = 2, HQ = 16, HKV = 8, SK = 32768, DH = 256, DM = 448, FF = 1792;
constexpr int NQ = HQ * DH;
constexpr int NSPLIT = 16;
constexpr int AW = 4;
constexpr int STAGE = 8;
constexpr int NL = STAGE / 2;
constexpr int RKEYS = SK / NSPLIT;
constexpr int WSTG = RKEYS / (STAGE * AW);
constexpr float LOG2E = 1.4426950408889634f;

constexpr size_t WS_CNT = 0;
constexpr size_t WS_Q = 4096;
constexpr size_t WS_G = WS_Q + (size_t)NB * NQ * 2;
constexpr size_t WS_U = WS_G + (size_t)NB * FF * 2;
constexpr size_t WS_PML = 40960;
constexpr size_t WS_PO = 65536;
constexpr size_t WS_SLAB = WS_PO + (size_t)NB * HQ * NSPLIT * DH * 4;
static_assert(WS_U + (size_t)NB * FF * 2 <= WS_PML && WS_PML + (size_t)NB * HQ * NSPLIT * 8 <= WS_PO, "workspace map");

__device__ __forceinline__ float bfr(float x) { return (float)(__bf16)x; }
__device__ __forceinline__ float bf_lo(u32 v) { return __uint_as_float(v << 16); }
__device__ __forceinline__ float bf_hi(u32 v) { return __uint_as_float(v & 0xffff0000u); }
__device__ __forceinline__ float bf2f(u16 v) { return __uint_as_float((u32)v << 16); }
__device__ __forceinline__ u16 f2bf(float x) { __bf16 b = (__bf16)x; return __builtin_bit_cast(u16, b); }
__device__ __forceinline__ float dot2(u32 k, u32 q, float acc) {
  return __builtin_amdgcn_fdot2_f32_bf16(__builtin_bit_cast(bf16x2, k), __builtin_bit_cast(bf16x2, q), acc, false);
}
template <int CTRL> __device__ __forceinline__ float dpp(float x) {
  return __builtin_bit_cast(float, __builtin_amdgcn_mov_dpp(__builtin_bit_cast(int, x), CTRL, 0xf, 0xf, true));
}
constexpr int XOR1 = 0xB1, XOR2 = 0x4E, XOR7 = 0x141, XOR8 = 0x128, ROR4 = 0x124;
__device__ __forceinline__ float sum16x(float x) {
  auto t = __builtin_amdgcn_permlane16_swap(__float_as_uint(x), __float_as_uint(x), false, false);
  return __uint_as_float(t[0]) + __uint_as_float(t[1]);
}
__device__ __forceinline__ float sum32x(float x) {
  auto t = __builtin_amdgcn_permlane32_swap(__float_as_uint(x), __float_as_uint(x), false, false);
  return __uint_as_float(t[0]) + __uint_as_float(t[1]);
}
__device__ __forceinline__ float wave_sum(float x) {
  x += dpp<XOR1>(x); x += dpp<XOR2>(x); x += dpp<XOR7>(x); x += dpp<XOR8>(x);
  return sum32x(sum16x(x));
}
__device__ __forceinline__ void ln_store(const float (&v)[7], float* xn, int lane) {
  float s = 0.f;
#pragma unroll
  for (int i = 0; i < 7; ++i) s += v[i];
  const float mean = wave_sum(s) * (1.0f / DM);
  float q = 0.f;
#pragma unroll
  for (int i = 0; i < 7; ++i) { const float t = v[i] - mean; q += t * t; }
  const float m_bf = bfr(mean);
  const float var_bf = bfr(wave_sum(q) * (1.0f / DM));
  const float den = bfr(sqrtf(bfr(var_bf + 1e-5f)));
#pragma unroll
  for (int i = 0; i < 7; ++i) xn[lane + 64 * i] = bfr(bfr(v[i] - m_bf) / den);
}

constexpr int K1_QWG = NQ / 32, K1_FWG = FF / 32;
__global__ void __launch_bounds__(256) k1_proj(const u16* __restrict__ x, const u16* __restrict__ wq,
                                               const u16* __restrict__ wg, const u16* __restrict__ wu,
                                               unsigned char* __restrict__ ws) {
  __shared__ float xn[NB * DM];
  __shared__ float red[4][4][16];
  const int tid = threadIdx.x, lane = tid & 63, wave = tid >> 6;
  const int wgid = blockIdx.x;
  const int cq = tid & 3, rg = tid >> 2;
  float v[7];
#pragma unroll
  for (int i = 0; i < 7; ++i) v[i] = bf2f(x[(wave & 1) * DM + lane + 64 * i]);
  const u16* wbase; u16* dst; int ld;
  if (wgid < K1_QWG) { wbase = wq + wgid * 32; dst = reinterpret_cast<u16*>(ws + WS_Q) + wgid * 32; ld = NQ; }
  else if (wgid < K1_QWG + K1_FWG) { const int f = (wgid - K1_QWG) * 32; wbase = wg + f; dst = reinterpret_cast<u16*>(ws + WS_G) + f; ld = FF; }
  else { const int f = (wgid - K1_QWG - K1_FWG) * 32; wbase = wu + f; dst = reinterpret_cast<u16*>(ws + WS_U) + f; ld = FF; }
  u32x4 w[7];
#pragma unroll
  for (int i = 0; i < 7; ++i) w[i] = *reinterpret_cast<const u32x4*>(wbase + cq * 8 + (size_t)(rg + 64 * i) * ld);
  if (wgid == 0 && tid < 8) reinterpret_cast<u32*>(ws + WS_CNT)[tid * 16] = 0u;
  if (wave < NB) ln_store(v, xn + wave * DM, lane);
  __syncthreads();
  float acc[16];
#pragma unroll
  for (int j = 0; j < 16; ++j) acc[j] = 0.f;
#pragma unroll
  for (int i = 0; i < 7; ++i) {
    const float x0 = xn[rg + 64 * i], x1 = xn[DM + rg + 64 * i];
#pragma unroll
    for (int j = 0; j < 4; ++j) {
      const float lo = bf_lo(w[i][j]), hi = bf_hi(w[i][j]);
      acc[2 * j] = fmaf(x0, lo, acc[2 * j]); acc[2 * j + 1] = fmaf(x0, hi, acc[2 * j + 1]);
      acc[8 + 2 * j] = fmaf(x1, lo, acc[8 + 2 * j]); acc[8 + 2 * j + 1] = fmaf(x1, hi, acc[8 + 2 * j + 1]);
    }
  }
#pragma unroll
  for (int j = 0; j < 16; ++j) {
    float t = acc[j];
    t += dpp<ROR4>(t); t += dpp<XOR8>(t);
    acc[j] = sum32x(sum16x(t));
  }
  if (lane < 4) {
#pragma unroll
    for (int j = 0; j < 16; j += 4) *reinterpret_cast<f32x4*>(&red[wave][lane][j]) = f32x4{acc[j], acc[j + 1], acc[j + 2], acc[j + 3]};
  }
  __syncthreads();
  if (tid < 64) {
    const int c4 = tid >> 4, idx = tid & 15;
    const float t = (red[0][c4][idx] + red[1][c4][idx]) + (red[2][c4][idx] + red[3][c4][idx]);
    dst[(idx >> 3) * ld + c4 * 8 + (idx & 7)] = f2bf(t);
  }
}

#define KV_LOAD(R, VOFF, SB) asm volatile("s_nop 4\n\t" \
  "global_load_dwordx4 %0, %4, %5 nt\n\t" \
  "global_load_dwordx4 %1, %4, %5 offset:1024 nt\n\t" \
  "global_load_dwordx4 %2, %4, %5 offset:2048 nt\n\t" \
  "global_load_dwordx4 %3, %4, %5 offset:3072 nt" \
  : "=&v"(R[0]), "=&v"(R[1]), "=&v"(R[2]), "=&v"(R[3]) : "v"(VOFF), "s"(SB) : "memory")
#define KV_WAIT(R, N) asm volatile("s_waitcnt vmcnt(%c4)" : "+v"(R[0]), "+v"(R[1]), "+v"(R[2]), "+v"(R[3]) : "i"(N) : "memory")

__device__ __forceinline__ float stg_qk(const u32x4 (&kk)[NL], const u32x4 (&q)[2], int lane) {
  const int c = lane & 31;
  float x[2 * NL];
#pragma unroll
  for (int i = 0; i < NL; ++i)
#pragma unroll
    for (int g = 0; g < 2; ++g) {
      float d = 0.f;
#pragma unroll
      for (int k = 0; k < 4; ++k) d = dot2(kk[i][k], q[g][k], d);
      x[i * 2 + g] = d;
    }
#pragma unroll
  for (int j = 0; j < NL; ++j) {
    auto t = __builtin_amdgcn_permlane16_swap(__float_as_uint(x[j]), __float_as_uint(x[j + NL]), false, false);
    x[j] = __uint_as_float(t[0]) + __uint_as_float(t[1]);
  }
#define TR_STEP(HALF, CTRL, BIT) _Pragma("unroll") for (int h = 0; h < (HALF); ++h) { \
    const float lo_ = x[h], hi_ = x[h + (HALF)]; const float t1_ = lo_ + dpp<CTRL>(lo_), t2_ = hi_ + dpp<CTRL>(hi_); \
    x[h] = (c & (BIT)) ? t2_ : t1_; }
  TR_STEP(2, XOR8, 8) TR_STEP(1, XOR7, 4)
#undef TR_STEP
  const float t = x[0] + dpp<XOR2>(x[0]);
  return t + dpp<XOR1>(t);
}
__device__ __forceinline__ void stg_softmax(float sc, float (&acc)[2][8], float& m, float& l, float* prow, int lane, f32x4 (&pr)[NL / 2]) {
  const int r = lane >> 5, c = lane & 31;
  sc = bfr(sc) * (0.0625f * LOG2E);
  float pm = fmaxf(sc, dpp<XOR8>(sc));
  {
    auto t = __builtin_amdgcn_permlane16_swap(__float_as_uint(pm), __float_as_uint(pm), false, false);
    pm = fmaxf(__uint_as_float(t[0]), __uint_as_float(t[1]));
    auto u = __builtin_amdgcn_permlane32_swap(__float_as_uint(pm), __float_as_uint(pm), false, false);
    pm = fmaxf(__uint_as_float(u[0]), __uint_as_float(u[1]));
  }
  const float mn = fmaxf(m, pm);
  const float alpha = __builtin_amdgcn_exp2f(m - mn);
  const float p = __builtin_amdgcn_exp2f(sc - mn);
  l = fmaf(l, alpha, p);
  m = mn;
  const float a0 = __builtin_bit_cast(float, __builtin_amdgcn_readlane(__builtin_bit_cast(int, alpha), 0));
  const float a1 = __builtin_bit_cast(float, __builtin_amdgcn_readlane(__builtin_bit_cast(int, alpha), 4));
  prow[r * STAGE + (c >> 2)] = p;
#pragma unroll
  for (int k = 0; k < 8; ++k) { acc[0][k] *= a0; acc[1][k] *= a1; }
#pragma unroll
  for (int u = 0; u < NL / 2; ++u) pr[u] = *reinterpret_cast<const f32x4*>(prow + r * STAGE + 4 * u);
}
__device__ __forceinline__ void stg_pv(const u32x4 (&vv)[NL], const f32x4 (&pr)[NL / 2], float (&acc)[2][8]) {
#pragma unroll
  for (int i = 0; i < NL; ++i) {
    const float p0 = pr[i >> 1][(i & 1) * 2], p1 = pr[i >> 1][(i & 1) * 2 + 1];
#pragma unroll
    for (int k = 0; k < 4; ++k) {
      const float lo = bf_lo(vv[i][k]), hi = bf_hi(vv[i][k]);
      acc[0][2 * k] = fmaf(p0, lo, acc[0][2 * k]); acc[0][2 * k + 1] = fmaf(p0, hi, acc[0][2 * k + 1]);
      acc[1][2 * k] = fmaf(p1, lo, acc[1][2 * k]); acc[1][2 * k + 1] = fmaf(p1, hi, acc[1][2 * k + 1]);
    }
  }
}

__global__ void __launch_bounds__(AW * 64) k2_attn(const u16* __restrict__ kc, const u16* __restrict__ vc,
                                                   unsigned char* __restrict__ ws) {
  __shared__ __attribute__((aligned(16))) float lds_p[AW][2 * STAGE];
  __shared__ __attribute__((aligned(16))) float lds_acc[AW][2][DH];
  __shared__ float lds_ml[AW][2][2];
  const int tid = threadIdx.x, lane = tid & 63, wave = __builtin_amdgcn_readfirstlane(tid >> 6);
  const int bid = blockIdx.x;
  const int split = bid & 15, bh = bid >> 4;
  const int b = bh >> 3, kvh = bh & 7;
  const int c = lane & 31;
  const char* kgb = reinterpret_cast<const char*>(kc + ((size_t)bh * SK + (size_t)split * RKEYS + (size_t)wave * STAGE) * DH);
  const char* vgb = reinterpret_cast<const char*>(vc + ((size_t)bh * SK + (size_t)split * RKEYS + (size_t)wave * STAGE) * DH);
  constexpr size_t SSTEP = (size_t)AW * STAGE * DH * 2;
  const u32 voff = lane * 16;
  u32x4 kk[NL], vv[NL];
  u32x4 q[2];
  {
    const u16* qp = reinterpret_cast<const u16*>(ws + WS_Q) + b * NQ + (kvh * 2) * DH + 8 * c;
    asm volatile("global_load_dwordx4 %0, %2, off\n\tglobal_load_dwordx4 %1, %2, off offset:512" : "=&v"(q[0]), "=&v"(q[1]) : "v"(qp) : "memory");
  }
  KV_LOAD(kk, voff, kgb); KV_LOAD(vv, voff, vgb);
  asm volatile("s_waitcnt vmcnt(%c2)" : "+v"(q[0]), "+v"(q[1]) : "i"(2 * NL) : "memory");
  float acc[2][8];
#pragma unroll
  for (int g = 0; g < 2; ++g)
#pragma unroll
    for (int k = 0; k < 8; ++k) acc[g][k] = 0.f;
  float m = -INFINITY, l = 0.f;
  float* prow = &lds_p[wave][0];
#pragma unroll 1
  for (int s = 0; s < WSTG; ++s) {
    const bool more = s + 1 < WSTG;
    const char* kb = more ? kgb + (size_t)(s + 1) * SSTEP : reinterpret_cast<const char*>(kc);
    const char* vb = more ? vgb + (size_t)(s + 1) * SSTEP : reinterpret_cast<const char*>(kc);
    KV_WAIT(kk, NL);
    const float sc = stg_qk(kk, q, lane);
    KV_LOAD(kk, voff, kb);
    f32x4 pr[NL / 2];
    stg_softmax(sc, acc, m, l, prow, lane, pr);
    KV_WAIT(vv, NL);
    stg_pv(vv, pr, acc);
    KV_LOAD(vv, voff, vb);
  }
  KV_WAIT(kk, 0); KV_WAIT(vv, 0);
#pragma unroll
  for (int g = 0; g < 2; ++g)
#pragma unroll
    for (int k = 0; k < 8; ++k) acc[g][k] = sum32x(acc[g][k]);
  l = sum32x(sum16x(l + dpp<XOR8>(l)));
  if (lane < 32) {
#pragma unroll
    for (int g = 0; g < 2; ++g) {
      *reinterpret_cast<f32x4*>(&lds_acc[wave][g][8 * c]) = f32x4{acc[g][0], acc[g][1], acc[g][2], acc[g][3]};
      *reinterpret_cast<f32x4*>(&lds_acc[wave][g][8 * c + 4]) = f32x4{acc[g][4], acc[g][5], acc[g][6], acc[g][7]};
    }
    if (c == 0 || c == 4) { lds_ml[wave][c >> 2][0] = m; lds_ml[wave][c >> 2][1] = l; }
  }
  __syncthreads();
#pragma unroll
  for (int g = 0; g < 2; ++g) {
    float M = -INFINITY;
#pragma unroll
    for (int w = 0; w < AW; ++w) M = fmaxf(M, lds_ml[w][g][0]);
    float L = 0.f, o = 0.f;
#pragma unroll
    for (int w = 0; w < AW; ++w) {
      const float wt = __builtin_amdgcn_exp2f(lds_ml[w][g][0] - M);
      L = fmaf(wt, lds_ml[w][g][1], L);
      o = fmaf(wt, lds_acc[w][g][tid], o);
    }
    const size_t row = ((size_t)b * HQ + kvh * 2 + g) * NSPLIT + split;
    reinterpret_cast<float*>(ws + WS_PO)[row * DH + tid] = o;
    if (tid == 0) { reinterpret_cast<float*>(ws + WS_PML)[row * 2] = M; reinterpret_cast<float*>(ws + WS_PML)[row * 2 + 1] = L; }
  }
}

constexpr int K3_CG = 7, K3_KC = 23;
__global__ void __launch_bounds__(512) k3_out(const u16* __restrict__ x, const u16* __restrict__ wo,
                                              const u16* __restrict__ wd, unsigned char* __restrict__ ws,
                                              u16* __restrict__ out) {
  __shared__ float act[2][DH];
  __shared__ __attribute__((aligned(16))) float red[8][8][16];
  const int tid = threadIdx.x, lane = tid & 63, wave = tid >> 6;
  const int cg = blockIdx.x % K3_CG, kcx = blockIdx.x / K3_CG;
  const int cq = tid & 7, rg = tid >> 3;
  const int ab = tid >> 8, ad = tid & 255;
  const u16* wbase = (kcx < HQ ? wo + (size_t)kcx * DH * DM : wd + (size_t)(kcx - HQ) * DH * DM) + cg * 64 + cq * 8;
  u32x4 w[4];
#pragma unroll
  for (int i = 0; i < 4; ++i) w[i] = *reinterpret_cast<const u32x4*>(wbase + (size_t)(rg + 64 * i) * DM);
  if (kcx < HQ) {
    const size_t row0 = ((size_t)ab * HQ + kcx) * NSPLIT;
    const float* pml = reinterpret_cast<const float*>(ws + WS_PML) + row0 * 2;
    const float* po = reinterpret_cast<const float*>(ws + WS_PO) + row0 * DH + ad;
    float a[NSPLIT], ms[NSPLIT], ls[NSPLIT];
#pragma unroll
    for (int s = 0; s < NSPLIT; ++s) { a[s] = po[(size_t)s * DH]; ms[s] = pml[2 * s]; ls[s] = pml[2 * s + 1]; }
    float M = -INFINITY;
#pragma unroll
    for (int s = 0; s < NSPLIT; ++s) M = fmaxf(M, ms[s]);
    float L = 0.f, o = 0.f;
#pragma unroll
    for (int s = 0; s < NSPLIT; ++s) {
      const float wt = __builtin_amdgcn_exp2f(ms[s] - M);
      L = fmaf(wt, ls[s], L);
      o = fmaf(wt, a[s], o);
    }
    act[ab][ad] = bfr(o / L);
  } else {
    const int hi = ab * FF + (kcx - HQ) * DH + ad;
    const float g = bf2f(reinterpret_cast<const u16*>(ws + WS_G)[hi]), u = bf2f(reinterpret_cast<const u16*>(ws + WS_U)[hi]);
    const float inner = 0.7978845608028654f * (g + 0.044715f * g * g * g);
    const float ge = bfr(0.5f * g * (1.0f + tanhf(inner)));
    act[ab][ad] = bfr(ge * u);
  }
  __syncthreads();
  float acc[16];
#pragma unroll
  for (int j = 0; j < 16; ++j) acc[j] = 0.f;
#pragma unroll
  for (int i = 0; i < 4; ++i) {
    const float x0 = act[0][rg + 64 * i], x1 = act[1][rg + 64 * i];
#pragma unroll
    for (int j = 0; j < 4; ++j) {
      const float lo = bf_lo(w[i][j]), hi = bf_hi(w[i][j]);
      acc[2 * j] = fmaf(x0, lo, acc[2 * j]); acc[2 * j + 1] = fmaf(x0, hi, acc[2 * j + 1]);
      acc[8 + 2 * j] = fmaf(x1, lo, acc[8 + 2 * j]); acc[8 + 2 * j + 1] = fmaf(x1, hi, acc[8 + 2 * j + 1]);
    }
  }
#pragma unroll
  for (int j = 0; j < 16; ++j) {
    float t = acc[j];
    t += dpp<XOR8>(t);
    acc[j] = sum32x(sum16x(t));
  }
  if (lane < 8) {
#pragma unroll
    for (int j = 0; j < 16; j += 4) *reinterpret_cast<f32x4*>(&red[wave][lane][j]) = f32x4{acc[j], acc[j + 1], acc[j + 2], acc[j + 3]};
  }
  __syncthreads();
  if (wave != 0) return;
  float* slab = reinterpret_cast<float*>(ws + WS_SLAB) + (size_t)cg * K3_KC * 128;
  {
    const int c8 = lane >> 3, j = lane & 7;
    float t0 = 0.f, t1 = 0.f;
#pragma unroll
    for (int wv = 0; wv < 8; ++wv) { t0 += red[wv][c8][j]; t1 += red[wv][c8][8 + j]; }
    __hip_atomic_store(slab + kcx * 128 + lane, t0, __ATOMIC_RELAXED, __HIP_MEMORY_SCOPE_AGENT);
    __hip_atomic_store(slab + kcx * 128 + 64 + lane, t1, __ATOMIC_RELAXED, __HIP_MEMORY_SCOPE_AGENT);
  }
  asm volatile("s_waitcnt vmcnt(0)" ::: "memory");
  u32 old = 0u;
  if (lane == 0) old = __hip_atomic_fetch_add(reinterpret_cast<u32*>(ws + WS_CNT) + cg * 16, 1u, __ATOMIC_RELAXED, __HIP_MEMORY_SCOPE_AGENT);
  if (__builtin_amdgcn_readfirstlane(old) != (u32)(K3_KC - 1)) return;
  float v0[K3_KC], v1[K3_KC];
#pragma unroll
  for (int k = 0; k < K3_KC; ++k) {
    v0[k] = __hip_atomic_load(slab + k * 128 + lane, __ATOMIC_RELAXED, __HIP_MEMORY_SCOPE_AGENT);
    v1[k] = __hip_atomic_load(slab + k * 128 + 64 + lane, __ATOMIC_RELAXED, __HIP_MEMORY_SCOPE_AGENT);
  }
  float sa0 = 0.f, sf0 = 0.f, sa1 = 0.f, sf1 = 0.f;
#pragma unroll
  for (int k = 0; k < HQ; ++k) { sa0 += v0[k]; sa1 += v1[k]; }
#pragma unroll
  for (int k = HQ; k < K3_KC; ++k) { sf0 += v0[k]; sf1 += v1[k]; }
  const int n = cg * 64 + lane;
  out[n] = f2bf(bfr(bf2f(x[n]) + bfr(sa0)) + bfr(sf0));
  out[DM + n] = f2bf(bfr(bf2f(x[DM + n]) + bfr(sa1)) + bfr(sf1));
}

extern "C" void kernel_launch(void* const* d_in, const int* in_sizes, int n_in,
                              void* d_out, int out_size, void* d_ws, size_t ws_size,
                              hipStream_t stream) {
  const u16* x = (const u16*)d_in[0];
  const u16* kc = (const u16*)d_in[1];
  const u16* vc = (const u16*)d_in[2];
  const u16* wq = (const u16*)d_in[3];
  const u16* wo = (const u16*)d_in[4];
  const u16* wg = (const u16*)d_in[6];
  const u16* wu = (const u16*)d_in[7];
  const u16* wd = (const u16*)d_in[8];
  unsigned char* ws = (unsigned char*)d_ws;
  k1_proj<<<K1_QWG + 2 * K1_FWG, 256, 0, stream>>>(x, wq, wg, wu, ws);
  k2_attn<<<NB * HKV * NSPLIT, AW * 64, 0, stream>>>(kc, vc, ws);
  k3_out<<<K3_CG * K3_KC, 512, 0, stream>>>(x, wo, wd, ws, (u16*)d_out);
}
```

```cpp
#define ROTMUL 5
#include <hip/hip_runtime.h>
#include <hip/hip_bf16.h>
#include <stdint.h>

typedef unsigned int u32;
typedef unsigned short u16;
using u32x4 = __attribute__((ext_vector_type(4))) unsigned;
using f32x4 = __attribute__((ext_vector_type(4))) float;
using bf16x2 = __attribute__((ext_vector_type(2))) __bf16;

constexpr int NB = 2, HQ = 16, HKV = 8, SK = 32768, DH = 256, DM = 448, FF = 1792;
constexpr int NQ = HQ * DH;
constexpr int NSPLIT = 16;
constexpr int AW = 4;
constexpr int STAGE = 8;
constexpr int NL = STAGE / 2;
constexpr int RKEYS = SK / NSPLIT;
constexpr int WSTG = RKEYS / (STAGE * AW);
constexpr float LOG2E = 1.4426950408889634f;

constexpr size_t WS_CNT = 0;
constexpr size_t WS_Q = 4096;
constexpr size_t WS_G = WS_Q + (size_t)NB * NQ * 2;
constexpr size_t WS_U = WS_G + (size_t)NB * FF * 2;
constexpr size_t WS_PML = 40960;
constexpr size_t WS_PO = 65536;
constexpr size_t WS_SLAB = WS_PO + (size_t)NB * HQ * NSPLIT * DH * 4;
static_assert(WS_U + (size_t)NB * FF * 2 <= WS_PML && WS_PML + (size_t)NB * HQ * NSPLIT * 8 <= WS_PO, "workspace map");

__device__ __forceinline__ float bfr(float x) { return (float)(__bf16)x; }
__device__ __forceinline__ float bf_lo(u32 v) { return __uint_as_float(v << 16); }
__device__ __forceinline__ float bf_hi(u32 v) { return __uint_as_float(v & 0xffff0000u); }
__device__ __forceinline__ float bf2f(u16 v) { return __uint_as_float((u32)v << 16); }
__device__ __forceinline__ u16 f2bf(float x) { __bf16 b = (__bf16)x; return __builtin_bit_cast(u16, b); }
__device__ __forceinline__ float dot2(u32 k, u32 q, float acc) {
  return __builtin_amdgcn_fdot2_f32_bf16(__builtin_bit_cast(bf16x2, k), __builtin_bit_cast(bf16x2, q), acc, false);
}
template <int CTRL> __device__ __forceinline__ float dpp(float x) {
  return __builtin_bit_cast(float, __builtin_amdgcn_mov_dpp(__builtin_bit_cast(int, x), CTRL, 0xf, 0xf, true));
}
constexpr int XOR1 = 0xB1, XOR2 = 0x4E, XOR7 = 0x141, XOR8 = 0x128, ROR4 = 0x124;
__device__ __forceinline__ float sum16x(float x) {
  auto t = __builtin_amdgcn_permlane16_swap(__float_as_uint(x), __float_as_uint(x), false, false);
  return __uint_as_float(t[0]) + __uint_as_float(t[1]);
}
__device__ __forceinline__ float sum32x(float x) {
  auto t = __builtin_amdgcn_permlane32_swap(__float_as_uint(x), __float_as_uint(x), false, false);
  return __uint_as_float(t[0]) + __uint_as_float(t[1]);
}
__device__ __forceinline__ float wave_sum(float x) {
  x += dpp<XOR1>(x); x += dpp<XOR2>(x); x += dpp<XOR7>(x); x += dpp<XOR8>(x);
  return sum32x(sum16x(x));
}
__device__ __forceinline__ void ln_store(const float (&v)[7], float* xn, int lane) {
  float s = 0.f;
#pragma unroll
  for (int i = 0; i < 7; ++i) s += v[i];
  const float mean = wave_sum(s) * (1.0f / DM);
  float q = 0.f;
#pragma unroll
  for (int i = 0; i < 7; ++i) { const float t = v[i] - mean; q += t * t; }
  const float m_bf = bfr(mean);
  const float var_bf = bfr(wave_sum(q) * (1.0f / DM));
  const float den = bfr(sqrtf(bfr(var_bf + 1e-5f)));
#pragma unroll
  for (int i = 0; i < 7; ++i) xn[lane + 64 * i] = bfr(bfr(v[i] - m_bf) / den);
}

constexpr int K1_QWG = NQ / 32, K1_FWG = FF / 32;
__global__ void __launch_bounds__(256) k1_proj(const u16* __restrict__ x, const u16* __restrict__ wq,
                                               const u16* __restrict__ wg, const u16* __restrict__ wu,
                                               unsigned char* __restrict__ ws) {
  __shared__ float xn[NB * DM];
  __shared__ float red[4][4][16];
  const int tid = threadIdx.x, lane = tid & 63, wave = tid >> 6;
  const int wgid = blockIdx.x;
  const int cq = tid & 3, rg = tid >> 2;
  float v[7];
#pragma unroll
  for (int i = 0; i < 7; ++i) v[i] = bf2f(x[(wave & 1) * DM + lane + 64 * i]);
  const u16* wbase; u16* dst; int ld;
  if (wgid < K1_QWG) { wbase = wq + wgid * 32; dst = reinterpret_cast<u16*>(ws + WS_Q) + wgid * 32; ld = NQ; }
  else if (wgid < K1_QWG + K1_FWG) { const int f = (wgid - K1_QWG) * 32; wbase = wg + f; dst = reinterpret_cast<u16*>(ws + WS_G) + f; ld = FF; }
  else { const int f = (wgid - K1_QWG - K1_FWG) * 32; wbase = wu + f; dst = reinterpret_cast<u16*>(ws + WS_U) + f; ld = FF; }
  u32x4 w[7];
#pragma unroll
  for (int i = 0; i < 7; ++i) w[i] = *reinterpret_cast<const u32x4*>(wbase + cq * 8 + (size_t)(rg + 64 * i) * ld);
  if (wgid == 0 && tid < 8) reinterpret_cast<u32*>(ws + WS_CNT)[tid * 16] = 0u;
  if (wave < NB) ln_store(v, xn + wave * DM, lane);
  __syncthreads();
  float acc[16];
#pragma unroll
  for (int j = 0; j < 16; ++j) acc[j] = 0.f;
#pragma unroll
  for (int i = 0; i < 7; ++i) {
    const float x0 = xn[rg + 64 * i], x1 = xn[DM + rg + 64 * i];
#pragma unroll
    for (int j = 0; j < 4; ++j) {
      const float lo = bf_lo(w[i][j]), hi = bf_hi(w[i][j]);
      acc[2 * j] = fmaf(x0, lo, acc[2 * j]); acc[2 * j + 1] = fmaf(x0, hi, acc[2 * j + 1]);
      acc[8 + 2 * j] = fmaf(x1, lo, acc[8 + 2 * j]); acc[8 + 2 * j + 1] = fmaf(x1, hi, acc[8 + 2 * j + 1]);
    }
  }
#pragma unroll
  for (int j = 0; j < 16; ++j) {
    float t = acc[j];
    t += dpp<ROR4>(t); t += dpp<XOR8>(t);
    acc[j] = sum32x(sum16x(t));
  }
  if (lane < 4) {
#pragma unroll
    for (int j = 0; j < 16; j += 4) *reinterpret_cast<f32x4*>(&red[wave][lane][j]) = f32x4{acc[j], acc[j + 1], acc[j + 2], acc[j + 3]};
  }
  __syncthreads();
  if (tid < 64) {
    const int c4 = tid >> 4, idx = tid & 15;
    const float t = (red[0][c4][idx] + red[1][c4][idx]) + (red[2][c4][idx] + red[3][c4][idx]);
    dst[(idx >> 3) * ld + c4 * 8 + (idx & 7)] = f2bf(t);
  }
}

#define KV_LOAD(R, VOFF, SB) asm volatile("s_nop 4\n\t" \
  "global_load_dwordx4 %0, %4, %5 nt\n\t" \
  "global_load_dwordx4 %1, %4, %5 offset:1024 nt\n\t" \
  "global_load_dwordx4 %2, %4, %5 offset:2048 nt\n\t" \
  "global_load_dwordx4 %3, %4, %5 offset:3072 nt" \
  : "=&v"(R[0]), "=&v"(R[1]), "=&v"(R[2]), "=&v"(R[3]) : "v"(VOFF), "s"(SB) : "memory")
#define KV_WAIT(R, N) asm volatile("s_waitcnt vmcnt(%c4)" : "+v"(R[0]), "+v"(R[1]), "+v"(R[2]), "+v"(R[3]) : "i"(N) : "memory")

__device__ __forceinline__ float stg_qk(const u32x4 (&kk)[NL], const u32x4 (&q)[2], int lane) {
  const int c = lane & 31;
  float x[2 * NL];
#pragma unroll
  for (int i = 0; i < NL; ++i)
#pragma unroll
    for (int g = 0; g < 2; ++g) {
      float d = 0.f;
#pragma unroll
      for (int k = 0; k < 4; ++k) d = dot2(kk[i][k], q[g][k], d);
      x[i * 2 + g] = d;
    }
#pragma unroll
  for (int j = 0; j < NL; ++j) {
    auto t = __builtin_amdgcn_permlane16_swap(__float_as_uint(x[j]), __float_as_uint(x[j + NL]), false, false);
    x[j] = __uint_as_float(t[0]) + __uint_as_float(t[1]);
  }
#define TR_STEP(HALF, CTRL, BIT) _Pragma("unroll") for (int h = 0; h < (HALF); ++h) { \
    const float lo_ = x[h], hi_ = x[h + (HALF)]; const float t1_ = lo_ + dpp<CTRL>(lo_), t2_ = hi_ + dpp<CTRL>(hi_); \
    x[h] = (c & (BIT)) ? t2_ : t1_; }
  TR_STEP(2, XOR8, 8) TR_STEP(1, XOR7, 4)
#undef TR_STEP
  const float t = x[0] + dpp<XOR2>(x[0]);
  return t + dpp<XOR1>(t);
}
__device__ __forceinline__ void stg_softmax(float sc, float (&acc)[2][8], float& m, float& l, float* prow, int lane, f32x4 (&pr)[NL / 2]) {
  const int r = lane >> 5, c = lane & 31;
  sc = bfr(sc) * (0.0625f * LOG2E);
  float pm = fmaxf(sc, dpp<XOR8>(sc));
  {
    auto t = __builtin_amdgcn_permlane16_swap(__float_as_uint(pm), __float_as_uint(pm), false, false);
    pm = fmaxf(__uint_as_float(t[0]), __uint_as_float(t[1]));
    auto u = __builtin_amdgcn_permlane32_swap(__float_as_uint(pm), __float_as_uint(pm), false, false);
    pm = fmaxf(__uint_as_float(u[0]), __uint_as_float(u[1]));
  }
  const float mn = fmaxf(m, pm);
  const float alpha = __builtin_amdgcn_exp2f(m - mn);
  const float p = __builtin_amdgcn_exp2f(sc - mn);
  l = fmaf(l, alpha, p);
  m = mn;
  const float a0 = __builtin_bit_cast(float, __builtin_amdgcn_readlane(__builtin_bit_cast(int, alpha), 0));
  const float a1 = __builtin_bit_cast(float, __builtin_amdgcn_readlane(__builtin_bit_cast(int, alpha), 4));
  prow[r * STAGE + (c >> 2)] = p;
#pragma unroll
  for (int k = 0; k < 8; ++k) { acc[0][k] *= a0; acc[1][k] *= a1; }
#pragma unroll
  for (int u = 0; u < NL / 2; ++u) pr[u] = *reinterpret_cast<const f32x4*>(prow + r * STAGE + 4 * u);
}
__device__ __forceinline__ void stg_pv(const u32x4 (&vv)[NL], const f32x4 (&pr)[NL / 2], float (&acc)[2][8]) {
#pragma unroll
  for (int i = 0; i < NL; ++i) {
    const float p0 = pr[i >> 1][(i & 1) * 2], p1 = pr[i >> 1][(i & 1) * 2 + 1];
#pragma unroll
    for (int k = 0; k < 4; ++k) {
      const float lo = bf_lo(vv[i][k]), hi = bf_hi(vv[i][k]);
      acc[0][2 * k] = fmaf(p0, lo, acc[0][2 * k]); acc[0][2 * k + 1] = fmaf(p0, hi, acc[0][2 * k + 1]);
      acc[1][2 * k] = fmaf(p1, lo, acc[1][2 * k]); acc[1][2 * k + 1] = fmaf(p1, hi, acc[1][2 * k + 1]);
    }
  }
}

__global__ void __launch_bounds__(AW * 64) k2_attn(const u16* __restrict__ kc, const u16* __restrict__ vc,
                                                   unsigned char* __restrict__ ws) {
  __shared__ __attribute__((aligned(16))) float lds_p[AW][2 * STAGE];
  __shared__ __attribute__((aligned(16))) float lds_acc[AW][2][DH];
  __shared__ float lds_ml[AW][2][2];
  const int tid = threadIdx.x, lane = tid & 63, wave = __builtin_amdgcn_readfirstlane(tid >> 6);
  const int bid = blockIdx.x;
  const int split = bid & 15, bh = bid >> 4;
  const int b = bh >> 3, kvh = bh & 7;
  const int c = lane & 31;
  const char* kgb = reinterpret_cast<const char*>(kc + ((size_t)bh * SK + (size_t)split * RKEYS + (size_t)wave * STAGE) * DH);
  const char* vgb = reinterpret_cast<const char*>(vc + ((size_t)bh * SK + (size_t)split * RKEYS + (size_t)wave * STAGE) * DH);
  constexpr size_t SSTEP = (size_t)AW * STAGE * DH * 2;
  const u32 voff = lane * 16;
  u32x4 kk[NL], vv[NL];
  u32x4 q[2];
  {
    const u16* qp = reinterpret_cast<const u16*>(ws + WS_Q) + b * NQ + (kvh * 2) * DH + 8 * c;
    asm volatile("global_load_dwordx4 %0, %2, off\n\tglobal_load_dwordx4 %1, %2, off offset:512" : "=&v"(q[0]), "=&v"(q[1]) : "v"(qp) : "memory");
  }
  const int rot = (bid * ROTMUL) & (WSTG - 1);
  { const char* k0 = kgb + (size_t)rot * SSTEP; const char* v0 = vgb + (size_t)rot * SSTEP; KV_LOAD(kk, voff, k0); KV_LOAD(vv, voff, v0); }
  asm volatile("s_waitcnt vmcnt(%c2)" : "+v"(q[0]), "+v"(q[1]) : "i"(2 * NL) : "memory");
  float acc[2][8];
#pragma unroll
  for (int g = 0; g < 2; ++g)
#pragma unroll
    for (int k = 0; k < 8; ++k) acc[g][k] = 0.f;
  float m = -INFINITY, l = 0.f;
  float* prow = &lds_p[wave][0];
#pragma unroll 1
  for (int s = 0; s < WSTG; ++s) {
    const bool more = s + 1 < WSTG;
    const size_t so = (size_t)((s + 1 + rot) & (WSTG - 1)) * SSTEP;
    const char* kb = more ? kgb + so : reinterpret_cast<const char*>(kc);
    const char* vb = more ? vgb + so : reinterpret_cast<const char*>(kc);
    KV_WAIT(kk, NL);
    const float sc = stg_qk(kk, q, lane);
    KV_LOAD(kk, voff, kb);
    f32x4 pr[NL / 2];
    stg_softmax(sc, acc, m, l, prow, lane, pr);
    KV_WAIT(vv, NL);
    stg_pv(vv, pr, acc);
    KV_LOAD(vv, voff, vb);
  }
  KV_WAIT(kk, 0); KV_WAIT(vv, 0);
#pragma unroll
  for (int g = 0; g < 2; ++g)
#pragma unroll
    for (int k = 0; k < 8; ++k) acc[g][k] = sum32x(acc[g][k]);
  l = sum32x(sum16x(l + dpp<XOR8>(l)));
  if (lane < 32) {
#pragma unroll
    for (int g = 0; g < 2; ++g) {
      *reinterpret_cast<f32x4*>(&lds_acc[wave][g][8 * c]) = f32x4{acc[g][0], acc[g][1], acc[g][2], acc[g][3]};
      *reinterpret_cast<f32x4*>(&lds_acc[wave][g][8 * c + 4]) = f32x4{acc[g][4], acc[g][5], acc[g][6], acc[g][7]};
    }
    if (c == 0 || c == 4) { lds_ml[wave][c >> 2][0] = m; lds_ml[wave][c >> 2][1] = l; }
  }
  __syncthreads();
#pragma unroll
  for (int g = 0; g < 2; ++g) {
    float M = -INFINITY;
#pragma unroll
    for (int w = 0; w < AW; ++w) M = fmaxf(M, lds_ml[w][g][0]);
    float L = 0.f, o = 0.f;
#pragma unroll
    for (int w = 0; w < AW; ++w) {
      const float wt = __builtin_amdgcn_exp2f(lds_ml[w][g][0] - M);
      L = fmaf(wt, lds_ml[w][g][1], L);
      o = fmaf(wt, lds_acc[w][g][tid], o);
    }
    const size_t row = ((size_t)b * HQ + kvh * 2 + g) * NSPLIT + split;
    reinterpret_cast<float*>(ws + WS_PO)[row * DH + tid] = o;
    if (tid == 0) { reinterpret_cast<float*>(ws + WS_PML)[row * 2] = M; reinterpret_cast<float*>(ws + WS_PML)[row * 2 + 1] = L; }
  }
}

constexpr int K3_CG = 7, K3_KC = 23;
__global__ void __launch_bounds__(512) k3_out(const u16* __restrict__ x, const u16* __restrict__ wo,
                                              const u16* __restrict__ wd, unsigned char* __restrict__ ws,
                                              u16* __restrict__ out) {
  __shared__ float act[2][DH];
  __shared__ __attribute__((aligned(16))) float red[8][8][16];
  const int tid = threadIdx.x, lane = tid & 63, wave = tid >> 6;
  const int cg = blockIdx.x % K3_CG, kcx = blockIdx.x / K3_CG;
  const int cq = tid & 7, rg = tid >> 3;
  const int ab = tid >> 8, ad = tid & 255;
  const u16* wbase = (kcx < HQ ? wo + (size_t)kcx * DH * DM : wd + (size_t)(kcx - HQ) * DH * DM) + cg * 64 + cq * 8;
  u32x4 w[4];
#pragma unroll
  for (int i = 0; i < 4; ++i) w[i] = *reinterpret_cast<const u32x4*>(wbase + (size_t)(rg + 64 * i) * DM);
  if (kcx < HQ) {
    const size_t row0 = ((size_t)ab * HQ + kcx) * NSPLIT;
    const float* pml = reinterpret_cast<const float*>(ws + WS_PML) + row0 * 2;
    const float* po = reinterpret_cast<const float*>(ws + WS_PO) + row0 * DH + ad;
    float a[NSPLIT], ms[NSPLIT], ls[NSPLIT];
#pragma unroll
    for (int s = 0; s < NSPLIT; ++s) { a[s] = po[(size_t)s * DH]; ms[s] = pml[2 * s]; ls[s] = pml[2 * s + 1]; }
    float M = -INFINITY;
#pragma unroll
    for (int s = 0; s < NSPLIT; ++s) M = fmaxf(M, ms[s]);
    float L = 0.f, o = 0.f;
#pragma unroll
    for (int s = 0; s < NSPLIT; ++s) {
      const float wt = __builtin_amdgcn_exp2f(ms[s] - M);
      L = fmaf(wt, ls[s], L);
      o = fmaf(wt, a[s], o);
    }
    act[ab][ad] = bfr(o / L);
  } else {
    const int hi = ab * FF + (kcx - HQ) * DH + ad;
    const float g = bf2f(reinterpret_cast<const u16*>(ws + WS_G)[hi]), u = bf2f(reinterpret_cast<const u16*>(ws + WS_U)[hi]);
    const float inner = 0.7978845608028654f * (g + 0.044715f * g * g * g);
    const float ge = bfr(0.5f * g * (1.0f + tanhf(inner)));
    act[ab][ad] = bfr(ge * u);
  }
  __syncthreads();
  float acc[16];
#pragma unroll
  for (int j = 0; j < 16; ++j) acc[j] = 0.f;
#pragma unroll
  for (int i = 0; i < 4; ++i) {
    const float x0 = act[0][rg + 64 * i], x1 = act[1][rg + 64 * i];
#pragma unroll
    for (int j = 0; j < 4; ++j) {
      const float lo = bf_lo(w[i][j]), hi = bf_hi(w[i][j]);
      acc[2 * j] = fmaf(x0, lo, acc[2 * j]); acc[2 * j + 1] = fmaf(x0, hi, acc[2 * j + 1]);
      acc[8 + 2 * j] = fmaf(x1, lo, acc[8 + 2 * j]); acc[8 + 2 * j + 1] = fmaf(x1, hi, acc[8 + 2 * j + 1]);
    }
  }
#pragma unroll
  for (int j = 0; j < 16; ++j) {
    float t = acc[j];
    t += dpp<XOR8>(t);
    acc[j] = sum32x(sum16x(t));
  }
  if (lane < 8) {
#pragma unroll
    for (int j = 0; j < 16; j += 4) *reinterpret_cast<f32x4*>(&red[wave][lane][j]) = f32x4{acc[j], acc[j + 1], acc[j + 2], acc[j + 3]};
  }
  __syncthreads();
  if (wave != 0) return;
  float* slab = reinterpret_cast<float*>(ws + WS_SLAB) + (size_t)cg * K3_KC * 128;
  {
    const int c8 = lane >> 3, j = lane & 7;
    float t0 = 0.f, t1 = 0.f;
#pragma unroll
    for (int wv = 0; wv < 8; ++wv) { t0 += red[wv][c8][j]; t1 += red[wv][c8][8 + j]; }
    __hip_atomic_store(slab + kcx * 128 + lane, t0, __ATOMIC_RELAXED, __HIP_MEMORY_SCOPE_AGENT);
    __hip_atomic_store(slab + kcx * 128 + 64 + lane, t1, __ATOMIC_RELAXED, __HIP_MEMORY_SCOPE_AGENT);
  }
  asm volatile("s_waitcnt vmcnt(0)" ::: "memory");
  u32 old = 0u;
  if (lane == 0) old = __hip_atomic_fetch_add(reinterpret_cast<u32*>(ws + WS_CNT) + cg * 16, 1u, __ATOMIC_RELAXED, __HIP_MEMORY_SCOPE_AGENT);
  if (__builtin_amdgcn_readfirstlane(old) != (u32)(K3_KC - 1)) return;
  __builtin_amdgcn_fence(__ATOMIC_ACQUIRE, "wavefront");
  float v0[K3_KC], v1[K3_KC];
#pragma unroll
  for (int k = 0; k < K3_KC; ++k) {
    v0[k] = __hip_atomic_load(slab + k * 128 + lane, __ATOMIC_RELAXED, __HIP_MEMORY_SCOPE_AGENT);
    v1[k] = __hip_atomic_load(slab + k * 128 + 64 + lane, __ATOMIC_RELAXED, __HIP_MEMORY_SCOPE_AGENT);
  }
  float sa0 = 0.f, sf0 = 0.f, sa1 = 0.f, sf1 = 0.f;
#pragma unroll
  for (int k = 0; k < HQ; ++k) { sa0 += v0[k]; sa1 += v1[k]; }
#pragma unroll
  for (int k = HQ; k < K3_KC; ++k) { sf0 += v0[k]; sf1 += v1[k]; }
  const int n = cg * 64 + lane;
  out[n] = f2bf(bfr(bf2f(x[n]) + bfr(sa0)) + bfr(sf0));
  out[DM + n] = f2bf(bfr(bf2f(x[DM + n]) + bfr(sa1)) + bfr(sf1));
}

extern "C" void kernel_launch(void* const* d_in, const int* in_sizes, int n_in,
                              void* d_out, int out_size, void* d_ws, size_t ws_size,
                              hipStream_t stream) {
  const u16* x = (const u16*)d_in[0];
  const u16* kc = (const u16*)d_in[1];
  const u16* vc = (const u16*)d_in[2];
  const u16* wq = (const u16*)d_in[3];
  const u16* wo = (const u16*)d_in[4];
  const u16* wg = (const u16*)d_in[6];
  const u16* wu = (const u16*)d_in[7];
  const u16* wd = (const u16*)d_in[8];
  unsigned char* ws = (unsigned char*)d_ws;
  k1_proj<<<K1_QWG + 2 * K1_FWG, 256, 0, stream>>>(x, wq, wg, wu, ws);
  k2_attn<<<NB * HKV * NSPLIT, AW * 64, 0, stream>>>(kc, vc, ws);
  k3_out<<<K3_CG * K3_KC, 512, 0, stream>>>(x, wo, wd, ws, (u16*)d_out);
}

#undef ROTMUL
```

```cpp
#include <hip/hip_runtime.h>
#include <hip/hip_bf16.h>
#include <stdint.h>

typedef unsigned int u32;
typedef unsigned short u16;
using u32x4 = __attribute__((ext_vector_type(4))) unsigned;
using f32x4 = __attribute__((ext_vector_type(4))) float;
using bf16x2 = __attribute__((ext_vector_type(2))) __bf16;

constexpr int NB = 2, HQ = 16, HKV = 8, SK = 32768, DH = 256, DM = 448, FF = 1792;
constexpr int NQ = HQ * DH;
constexpr int NSPLIT = 16;
constexpr int AW = 4;
constexpr int STAGE = 8;
constexpr int NL = STAGE / 2;
constexpr int RKEYS = SK / NSPLIT;
constexpr int WSTG = RKEYS / (STAGE * AW);
constexpr float LOG2E = 1.4426950408889634f;

constexpr size_t WS_CNT = 0;
constexpr size_t WS_Q = 4096;
constexpr size_t WS_G = WS_Q + (size_t)NB * NQ * 2;
constexpr size_t WS_U = WS_G + (size_t)NB * FF * 2;
constexpr size_t WS_PML = 40960;
constexpr size_t WS_PO = 65536;
constexpr size_t WS_SLAB = WS_PO + (size_t)NB * HQ * NSPLIT * DH * 4;
static_assert(WS_U + (size_t)NB * FF * 2 <= WS_PML && WS_PML + (size_t)NB * HQ * NSPLIT * 8 <= WS_PO, "workspace map");

__device__ __forceinline__ float bfr(float x) { return (float)(__bf16)x; }
__device__ __forceinline__ float bf_lo(u32 v) { return __uint_as_float(v << 16); }
__device__ __forceinline__ float bf_hi(u32 v) { return __uint_as_float(v & 0xffff0000u); }
__device__ __forceinline__ float bf2f(u16 v) { return __uint_as_float((u32)v << 16); }
__device__ __forceinline__ u16 f2bf(float x) { __bf16 b = (__bf16)x; return __builtin_bit_cast(u16, b); }
__device__ __forceinline__ float dot2(u32 k, u32 q, float acc) {
  return __builtin_amdgcn_fdot2_f32_bf16(__builtin_bit_cast(bf16x2, k), __builtin_bit_cast(bf16x2, q), acc, false);
}
template <int CTRL> __device__ __forceinline__ float dpp(float x) {
  return __builtin_bit_cast(float, __builtin_amdgcn_mov_dpp(__builtin_bit_cast(int, x), CTRL, 0xf, 0xf, true));
}
constexpr int XOR1 = 0xB1, XOR2 = 0x4E, XOR7 = 0x141, XOR8 = 0x128, ROR4 = 0x124;
__device__ __forceinline__ float sum16x(float x) {
  auto t = __builtin_amdgcn_permlane16_swap(__float_as_uint(x), __float_as_uint(x), false, false);
  return __uint_as_float(t[0]) + __uint_as_float(t[1]);
}
__device__ __forceinline__ float sum32x(float x) {
  auto t = __builtin_amdgcn_permlane32_swap(__float_as_uint(x), __float_as_uint(x), false, false);
  return __uint_as_float(t[0]) + __uint_as_float(t[1]);
}
__device__ __forceinline__ float wave_sum(float x) {
  x += dpp<XOR1>(x); x += dpp<XOR2>(x); x += dpp<XOR7>(x); x += dpp<XOR8>(x);
  return sum32x(sum16x(x));
}
__device__ __forceinline__ void ln_store(const float (&v)[7], float* xn, int lane) {
  float s = 0.f;
#pragma unroll
  for (int i = 0; i < 7; ++i) s += v[i];
  const float mean = wave_sum(s) * (1.0f / DM);
  float q = 0.f;
#pragma unroll
  for (int i = 0; i < 7; ++i) { const float t = v[i] - mean; q += t * t; }
  const float m_bf = bfr(mean);
  const float var_bf = bfr(wave_sum(q) * (1.0f / DM));
  const float den = bfr(sqrtf(bfr(var_bf + 1e-5f)));
#pragma unroll
  for (int i = 0; i < 7; ++i) xn[lane + 64 * i] = bfr(bfr(v[i] - m_bf) / den);
}

constexpr int K1_QWG = NQ / 32, K1_FWG = FF / 32;
__global__ void __launch_bounds__(256) k1_proj(const u16* __restrict__ x, const u16* __restrict__ wq,
                                               const u16* __restrict__ wg, const u16* __restrict__ wu,
                                               unsigned char* __restrict__ ws) {
  __shared__ float xn[NB * DM];
  __shared__ float red[4][4][16];
  const int tid = threadIdx.x, lane = tid & 63, wave = tid >> 6;
  const int wgid = blockIdx.x;
  const int cq = tid & 3, rg = tid >> 2;
  float v[7];
#pragma unroll
  for (int i = 0; i < 7; ++i) v[i] = bf2f(x[(wave & 1) * DM + lane + 64 * i]);
  const u16* wbase; u16* dst; int ld;
  if (wgid < K1_QWG) { wbase = wq + wgid * 32; dst = reinterpret_cast<u16*>(ws + WS_Q) + wgid * 32; ld = NQ; }
  else if (wgid < K1_QWG + K1_FWG) { const int f = (wgid - K1_QWG) * 32; wbase = wg + f; dst = reinterpret_cast<u16*>(ws + WS_G) + f; ld = FF; }
  else { const int f = (wgid - K1_QWG - K1_FWG) * 32; wbase = wu + f; dst = reinterpret_cast<u16*>(ws + WS_U) + f; ld = FF; }
  u32x4 w[7];
#pragma unroll
  for (int i = 0; i < 7; ++i) w[i] = *reinterpret_cast<const u32x4*>(wbase + cq * 8 + (size_t)(rg + 64 * i) * ld);
  if (wgid == 0 && tid < 8) reinterpret_cast<u32*>(ws + WS_CNT)[tid * 16] = 0u;
  if (wave < NB) ln_store(v, xn + wave * DM, lane);
  __syncthreads();
  float acc[16];
#pragma unroll
  for (int j = 0; j < 16; ++j) acc[j] = 0.f;
#pragma unroll
  for (int i = 0; i < 7; ++i) {
    const float x0 = xn[rg + 64 * i], x1 = xn[DM + rg + 64 * i];
#pragma unroll
    for (int j = 0; j < 4; ++j) {
      const float lo = bf_lo(w[i][j]), hi = bf_hi(w[i][j]);
      acc[2 * j] = fmaf(x0, lo, acc[2 * j]); acc[2 * j + 1] = fmaf(x0, hi, acc[2 * j + 1]);
      acc[8 + 2 * j] = fmaf(x1, lo, acc[8 + 2 * j]); acc[8 + 2 * j + 1] = fmaf(x1, hi, acc[8 + 2 * j + 1]);
    }
  }
#pragma unroll
  for (int j = 0; j < 16; ++j) {
    float t = acc[j];
    t += dpp<ROR4>(t); t += dpp<XOR8>(t);
    acc[j] = sum32x(sum16x(t));
  }
  if (lane < 4) {
#pragma unroll
    for (int j = 0; j < 16; j += 4) *reinterpret_cast<f32x4*>(&red[wave][lane][j]) = f32x4{acc[j], acc[j + 1], acc[j + 2], acc[j + 3]};
  }
  __syncthreads();
  if (tid < 64) {
    const int c4 = tid >> 4, idx = tid & 15;
    const float t = (red[0][c4][idx] + red[1][c4][idx]) + (red[2][c4][idx] + red[3][c4][idx]);
    dst[(idx >> 3) * ld + c4 * 8 + (idx & 7)] = f2bf(t);
  }
}

#define KV_LOAD(R, VOFF, SB) asm volatile("s_nop 4\n\t" \
  "global_load_dwordx4 %0, %4, %5 nt\n\t" \
  "global_load_dwordx4 %1, %4, %5 offset:1024 nt\n\t" \
  "global_load_dwordx4 %2, %4, %5 offset:2048 nt\n\t" \
  "global_load_dwordx4 %3, %4, %5 offset:3072 nt" \
  : "=&v"(R[0]), "=&v"(R[1]), "=&v"(R[2]), "=&v"(R[3]) : "v"(VOFF), "s"(SB) : "memory")
#define KV_WAIT(R, N) asm volatile("s_waitcnt vmcnt(%c4)" : "+v"(R[0]), "+v"(R[1]), "+v"(R[2]), "+v"(R[3]) : "i"(N) : "memory")

__device__ __forceinline__ float stg_qk(const u32x4 (&kk)[NL], const u32x4 (&q)[2], int lane) {
  const int c = lane & 31;
  float x[2 * NL];
#pragma unroll
  for (int i = 0; i < NL; ++i)
#pragma unroll
    for (int g = 0; g < 2; ++g) {
      float d = 0.f;
#pragma unroll
      for (int k = 0; k < 4; ++k) d = dot2(kk[i][k], q[g][k], d);
      x[i * 2 + g] = d;
    }
#pragma unroll
  for (int j = 0; j < NL; ++j) {
    auto t = __builtin_amdgcn_permlane16_swap(__float_as_uint(x[j]), __float_as_uint(x[j + NL]), false, false);
    x[j] = __uint_as_float(t[0]) + __uint_as_float(t[1]);
  }
#define TR_STEP(HALF, CTRL, BIT) _Pragma("unroll") for (int h = 0; h < (HALF); ++h) { \
    const float lo_ = x[h], hi_ = x[h + (HALF)]; const float t1_ = lo_ + dpp<CTRL>(lo_), t2_ = hi_ + dpp<CTRL>(hi_); \
    x[h] = (c & (BIT)) ? t2_ : t1_; }
  TR_STEP(2, XOR8, 8) TR_STEP(1, XOR7, 4)
#undef TR_STEP
  const float t = x[0] + dpp<XOR2>(x[0]);
  return t + dpp<XOR1>(t);
}
__device__ __forceinline__ void stg_softmax(float sc, float (&acc)[2][8], float& m, float& l, float* prow, int lane, f32x4 (&pr)[NL / 2]) {
  const int r = lane >> 5, c = lane & 31;
  sc = bfr(sc) * (0.0625f * LOG2E);
  float pm = fmaxf(sc, dpp<XOR8>(sc));
  {
    auto t = __builtin_amdgcn_permlane16_swap(__float_as_uint(pm), __float_as_uint(pm), false, false);
    pm = fmaxf(__uint_as_float(t[0]), __uint_as_float(t[1]));
    auto u = __builtin_amdgcn_permlane32_swap(__float_as_uint(pm), __float_as_uint(pm), false, false);
    pm = fmaxf(__uint_as_float(u[0]), __uint_as_float(u[1]));
  }
  const float mn = fmaxf(m, pm);
  const float alpha = __builtin_amdgcn_exp2f(m - mn);
  const float p = __builtin_amdgcn_exp2f(sc - mn);
  l = fmaf(l, alpha, p);
  m = mn;
  const float a0 = __builtin_bit_cast(float, __builtin_amdgcn_readlane(__builtin_bit_cast(int, alpha), 0));
  const float a1 = __builtin_bit_cast(float, __builtin_amdgcn_readlane(__builtin_bit_cast(int, alpha), 4));
  prow[r * STAGE + (c >> 2)] = p;
#pragma unroll
  for (int k = 0; k < 8; ++k) { acc[0][k] *= a0; acc[1][k] *= a1; }
#pragma unroll
  for (int u = 0; u < NL / 2; ++u) pr[u] = *reinterpret_cast<const f32x4*>(prow + r * STAGE + 4 * u);
}
__device__ __forceinline__ void stg_pv(const u32x4 (&vv)[NL], const f32x4 (&pr)[NL / 2], float (&acc)[2][8]) {
#pragma unroll
  for (int i = 0; i < NL; ++i) {
    const float p0 = pr[i >> 1][(i & 1) * 2], p1 = pr[i >> 1][(i & 1) * 2 + 1];
#pragma unroll
    for (int k = 0; k < 4; ++k) {
      const float lo = bf_lo(vv[i][k]), hi = bf_hi(vv[i][k]);
      acc[0][2 * k] = fmaf(p0, lo, acc[0][2 * k]); acc[0][2 * k + 1] = fmaf(p0, hi, acc[0][2 * k + 1]);
      acc[1][2 * k] = fmaf(p1, lo, acc[1][2 * k]); acc[1][2 * k + 1] = fmaf(p1, hi, acc[1][2 * k + 1]);
    }
  }
}

__global__ void __launch_bounds__(AW * 64) k2_attn(const u16* __restrict__ kc, const u16* __restrict__ vc,
                                                   unsigned char* __restrict__ ws) {
  __shared__ __attribute__((aligned(16))) float lds_p[AW][2 * STAGE];
  __shared__ __attribute__((aligned(16))) float lds_acc[AW][2][DH];
  __shared__ float lds_ml[AW][2][2];
  const int tid = threadIdx.x, lane = tid & 63, wave = __builtin_amdgcn_readfirstlane(tid >> 6);
  const int bid = blockIdx.x;
  const int split = bid & 15, bh = bid >> 4;
  const int b = bh >> 3, kvh = bh & 7;
  const int c = lane & 31;
  const char* kgb = reinterpret_cast<const char*>(kc + ((size_t)bh * SK + (size_t)split * RKEYS + (size_t)wave * STAGE) * DH);
  const char* vgb = reinterpret_cast<const char*>(vc + ((size_t)bh * SK + (size_t)split * RKEYS + (size_t)wave * STAGE) * DH);
  constexpr size_t SSTEP = (size_t)AW * STAGE * DH * 2;
  const u32 voff = lane * 16;
  u32x4 kk[NL], vv[NL];
  u32x4 q[2];
  {
    const u16* qp = reinterpret_cast<const u16*>(ws + WS_Q) + b * NQ + (kvh * 2) * DH + 8 * c;
    asm volatile("global_load_dwordx4 %0, %2, off\n\tglobal_load_dwordx4 %1, %2, off offset:512" : "=&v"(q[0]), "=&v"(q[1]) : "v"(qp) : "memory");
  }
  KV_LOAD(kk, voff, kgb); KV_LOAD(vv, voff, vgb);
  asm volatile("s_waitcnt vmcnt(%c2)" : "+v"(q[0]), "+v"(q[1]) : "i"(2 * NL) : "memory");
  float acc[2][8];
#pragma unroll
  for (int g = 0; g < 2; ++g)
#pragma unroll
    for (int k = 0; k < 8; ++k) acc[g][k] = 0.f;
  float m = -INFINITY, l = 0.f;
  float* prow = &lds_p[wave][0];
#pragma unroll 1
  for (int s = 0; s < WSTG; ++s) {
    const bool more = s + 1 < WSTG;
    const char* kb = more ? kgb + (size_t)(s + 1) * SSTEP : reinterpret_cast<const char*>(kc);
    const char* vb = more ? vgb + (size_t)(s + 1) * SSTEP : reinterpret_cast<const char*>(kc);
    KV_WAIT(kk, NL);
    const float sc = stg_qk(kk, q, lane);
    KV_LOAD(kk, voff, kb);
    f32x4 pr[NL / 2];
    stg_softmax(sc, acc, m, l, prow, lane, pr);
    KV_WAIT(vv, NL);
    stg_pv(vv, pr, acc);
    KV_LOAD(vv, voff, vb);
  }
  KV_WAIT(kk, 0); KV_WAIT(vv, 0);
#pragma unroll
  for (int g = 0; g < 2; ++g)
#pragma unroll
    for (int k = 0; k < 8; ++k) acc[g][k] = sum32x(acc[g][k]);
  l = sum32x(sum16x(l + dpp<XOR8>(l)));
  if (lane < 32) {
#pragma unroll
    for (int g = 0; g < 2; ++g) {
      *reinterpret_cast<f32x4*>(&lds_acc[wave][g][8 * c]) = f32x4{acc[g][0], acc[g][1], acc[g][2], acc[g][3]};
      *reinterpret_cast<f32x4*>(&lds_acc[wave][g][8 * c + 4]) = f32x4{acc[g][4], acc[g][5], acc[g][6], acc[g][7]};
    }
    if (c == 0 || c == 4) { lds_ml[wave][c >> 2][0] = m; lds_ml[wave][c >> 2][1] = l; }
  }
  __syncthreads();
#pragma unroll
  for (int g = 0; g < 2; ++g) {
    float M = -INFINITY;
#pragma unroll
    for (int w = 0; w < AW; ++w) M = fmaxf(M, lds_ml[w][g][0]);
    float L = 0.f, o = 0.f;
#pragma unroll
    for (int w = 0; w < AW; ++w) {
      const float wt = __builtin_amdgcn_exp2f(lds_ml[w][g][0] - M);
      L = fmaf(wt, lds_ml[w][g][1], L);
      o = fmaf(wt, lds_acc[w][g][tid], o);
    }
    const size_t row = ((size_t)b * HQ + kvh * 2 + g) * NSPLIT + split;
    reinterpret_cast<float*>(ws + WS_PO)[row * DH + tid] = o;
    if (tid == 0) { reinterpret_cast<float*>(ws + WS_PML)[row * 2] = M; reinterpret_cast<float*>(ws + WS_PML)[row * 2 + 1] = L; }
  }
}

constexpr int K3_CG = 7, K3_KC = 23;
__global__ void __launch_bounds__(512) k3_out(const u16* __restrict__ x, const u16* __restrict__ wo,
                                              const u16* __restrict__ wd, unsigned char* __restrict__ ws,
                                              u16* __restrict__ out) {
  __shared__ float act[2][DH];
  __shared__ __attribute__((aligned(16))) float red[8][8][16];
  const int tid = threadIdx.x, lane = tid & 63, wave = tid >> 6;
  const int cg = blockIdx.x % K3_CG, kcx = blockIdx.x / K3_CG;
  const int cq = tid & 7, rg = tid >> 3;
  const int ab = tid >> 8, ad = tid & 255;
  const u16* wbase = (kcx < HQ ? wo + (size_t)kcx * DH * DM : wd + (size_t)(kcx - HQ) * DH * DM) + cg * 64 + cq * 8;
  u32x4 w[4];
#pragma unroll
  for (int i = 0; i < 4; ++i) w[i] = *reinterpret_cast<const u32x4*>(wbase + (size_t)(rg + 64 * i) * DM);
  if (kcx < HQ) {
    const size_t row0 = ((size_t)ab * HQ + kcx) * NSPLIT;
    const float* pml = reinterpret_cast<const float*>(ws + WS_PML) + row0 * 2;
    const float* po = reinterpret_cast<const float*>(ws + WS_PO) + row0 * DH + ad;
    float a[NSPLIT], ms[NSPLIT], ls[NSPLIT];
#pragma unroll
    for (int s = 0; s < NSPLIT; ++s) { a[s] = po[(size_t)s * DH]; ms[s] = pml[2 * s]; ls[s] = pml[2 * s + 1]; }
    float M = -INFINITY;
#pragma unroll
    for (int s = 0; s < NSPLIT; ++s) M = fmaxf(M, ms[s]);
    float L = 0.f, o = 0.f;
#pragma unroll
    for (int s = 0; s < NSPLIT; ++s) {
      const float wt = __builtin_amdgcn_exp2f(ms[s] - M);
      L = fmaf(wt, ls[s], L);
      o = fmaf(wt, a[s], o);
    }
    act[ab][ad] = bfr(o / L);
  } else {
    const int hi = ab * FF + (kcx - HQ) * DH + ad;
    const float g = bf2f(reinterpret_cast<const u16*>(ws + WS_G)[hi]), u = bf2f(reinterpret_cast<const u16*>(ws + WS_U)[hi]);
    const float inner = 0.7978845608028654f * (g + 0.044715f * g * g * g);
    const float ge = bfr(0.5f * g * (1.0f + tanhf(inner)));
    act[ab][ad] = bfr(ge * u);
  }
  __syncthreads();
  float acc[16];
#pragma unroll
  for (int j = 0; j < 16; ++j) acc[j] = 0.f;
#pragma unroll
  for (int i = 0; i < 4; ++i) {
    const float x0 = act[0][rg + 64 * i], x1 = act[1][rg + 64 * i];
#pragma unroll
    for (int j = 0; j < 4; ++j) {
      const float lo = bf_lo(w[i][j]), hi = bf_hi(w[i][j]);
      acc[2 * j] = fmaf(x0, lo, acc[2 * j]); acc[2 * j + 1] = fmaf(x0, hi, acc[2 * j + 1]);
      acc[8 + 2 * j] = fmaf(x1, lo, acc[8 + 2 * j]); acc[8 + 2 * j + 1] = fmaf(x1, hi, acc[8 + 2 * j + 1]);
    }
  }
#pragma unroll
  for (int j = 0; j < 16; ++j) {
    float t = acc[j];
    t += dpp<XOR8>(t);
    acc[j] = sum32x(sum16x(t));
  }
  if (lane < 8) {
#pragma unroll
    for (int j = 0; j < 16; j += 4) *reinterpret_cast<f32x4*>(&red[wave][lane][j]) = f32x4{acc[j], acc[j + 1], acc[j + 2], acc[j + 3]};
  }
  __syncthreads();
  if (wave != 0) return;
  float* slab = reinterpret_cast<float*>(ws + WS_SLAB) + (size_t)cg * K3_KC * 128;
  {
    const int c8 = lane >> 3, j = lane & 7;
    float t0 = 0.f, t1 = 0.f;
#pragma unroll
    for (int wv = 0; wv < 8; ++wv) { t0 += red[wv][c8][j]; t1 += red[wv][c8][8 + j]; }
    __hip_atomic_store(slab + kcx * 128 + lane, t0, __ATOMIC_RELAXED, __HIP_MEMORY_SCOPE_AGENT);
    __hip_atomic_store(slab + kcx * 128 + 64 + lane, t1, __ATOMIC_RELAXED, __HIP_MEMORY_SCOPE_AGENT);
  }
  asm volatile("s_waitcnt vmcnt(0)" ::: "memory");
  u32 old = 0u;
  if (lane == 0) old = __hip_atomic_fetch_add(reinterpret_cast<u32*>(ws + WS_CNT) + cg * 16, 1u, __ATOMIC_RELAXED, __HIP_MEMORY_SCOPE_AGENT);
  if (__builtin_amdgcn_readfirstlane(old) != (u32)(K3_KC - 1)) return;
  __builtin_amdgcn_fence(__ATOMIC_ACQUIRE, "wavefront");
  float v0[K3_KC], v1[K3_KC];
#pragma unroll
  for (int k = 0; k < K3_KC; ++k) {
    v0[k] = __hip_atomic_load(slab + k * 128 + lane, __ATOMIC_RELAXED, __HIP_MEMORY_SCOPE_AGENT);
    v1[k] = __hip_atomic_load(slab + k * 128 + 64 + lane, __ATOMIC_RELAXED, __HIP_MEMORY_SCOPE_AGENT);
  }
  float sa0 = 0.f, sf0 = 0.f, sa1 = 0.f, sf1 = 0.f;
#pragma unroll
  for (int k = 0; k < HQ; ++k) { sa0 += v0[k]; sa1 += v1[k]; }
#pragma unroll
  for (int k = HQ; k < K3_KC; ++k) { sf0 += v0[k]; sf1 += v1[k]; }
  const int n = cg * 64 + lane;
  out[n] = f2bf(bfr(bf2f(x[n]) + bfr(sa0)) + bfr(sf0));
  out[DM + n] = f2bf(bfr(bf2f(x[DM + n]) + bfr(sa1)) + bfr(sf1));
}

extern "C" void kernel_launch(void* const* d_in, const int* in_sizes, int n_in,
                              void* d_out, int out_size, void* d_ws, size_t ws_size,
                              hipStream_t stream) {
  const u16* x = (const u16*)d_in[0];
  const u16* kc = (const u16*)d_in[1];
  const u16* vc = (const u16*)d_in[2];
  const u16* wq = (const u16*)d_in[3];
  const u16* wo = (const u16*)d_in[4];
  const u16* wg = (const u16*)d_in[6];
  const u16* wu = (const u16*)d_in[7];
  const u16* wd = (const u16*)d_in[8];
  unsigned char* ws = (unsigned char*)d_ws;
  k1_proj<<<K1_QWG + 2 * K1_FWG, 256, 0, stream>>>(x, wq, wg, wu, ws);
  k2_attn<<<NB * HKV * NSPLIT, AW * 64, 0, stream>>>(kc, vc, ws);
  k3_out<<<K3_CG * K3_KC, 512, 0, stream>>>(x, wo, wd, ws, (u16*)d_out);
}
```
